```python
import torch

def _softmax(x, axis=-1):
    return torch.softmax(x, dim=axis)
def _gelu(x):
    return torch.nn.functional.gelu(x, approximate="tanh")
def _silu(x):
    return torch.nn.functional.silu(x)
def _sigmoid(x):
    return torch.sigmoid(x)
def _layernorm(x, axis=-1, eps=1e-5):
    m = x.mean(dim=axis, keepdim=True)
    v = x.var(dim=axis, keepdim=True, unbiased=False)
    return (x - m) / torch.sqrt(v + eps)
def _scatter_add(tgt, idx, src, axis=0):
    return tgt.clone().index_add_(axis, idx, src)
def _segment_sum(src, seg, n):
    out = torch.zeros((n,) + src.shape[1:], dtype=src.dtype)
    return out.index_add_(0, seg, src)
def _pad(x, pw):
    flat = tuple(v for pair in reversed(pw) for v in pair)
    return torch.nn.functional.pad(x, flat)

kernel_name = 'parallel_sum__sliding_block_cached__mixer_channel_mlp__decode'

def setup_inputs():

    _w = (lambda a: (lambda t: t.to(torch.bfloat16) if t.is_floating_point() else t)(torch.as_tensor(a)))
    def _A_setup():
        B, Sq, D = 1, 1, 448
        Hq, Hkv, Sk, Dh = 16, 8, 32768, 256
        assert Hq % Hkv == 0, (Hq, Hkv)
        return {
            "x": _w((lambda *s: torch.randn(*s, dtype=torch.bfloat16) * 0.05)(B, Sq, D)),
            "k_cache": _w((lambda *s: torch.randn(*s, dtype=torch.bfloat16) * 3.2)(B, Hkv, Sk, Dh)),
            "v_cache": _w((lambda *s: torch.randn(*s, dtype=torch.bfloat16) * 0.4)(B, Hkv, Sk, Dh)),
            "wq": _w((lambda *s: torch.randn(*s, dtype=torch.bfloat16) * 3.2)(D, Hq * Dh) * (D ** -0.5)),
            "wo": _w((lambda *s: torch.randn(*s, dtype=torch.bfloat16) * 0.4)(Hq * Dh, D) * ((Hq * Dh) ** -0.5)),
        }
    _A_in = _A_setup()

    def _B_setup():
        B, S, D, F = 1, 1, 448, 1792
        return {
            "x": _w((lambda *s: torch.randn(*s, dtype=torch.bfloat16) * 0.05)(B, S, D)),
            "w1": _w((lambda *s: torch.randn(*s, dtype=torch.bfloat16) * 3.2)(D, F) * (D ** -0.5)),
            "b1": _w((lambda *s: torch.randn(*s, dtype=torch.bfloat16) * 1.6)(F)),
            "w2": _w((lambda *s: torch.randn(*s, dtype=torch.bfloat16) * 0.1)(F, D) * (F ** -0.5)),
            "b2": _w((lambda *s: torch.randn(*s, dtype=torch.bfloat16) * 0.1)(D)),
        }
    _B_in = _B_setup()

    return {
        "A_x": _A_in["x"],
        "A_k_cache": _A_in["k_cache"],
        "A_v_cache": _A_in["v_cache"],
        "A_wq": _A_in["wq"],
        "A_wo": _A_in["wo"],
        "B_x": _B_in["x"],
        "B_w1": _B_in["w1"],
        "B_b1": _B_in["b1"],
        "B_w2": _B_in["w2"],
        "B_b2": _B_in["b2"],
    }

def reference(A_x, A_k_cache, A_v_cache, A_wq, A_wo, B_x, B_w1, B_b1, B_w2, B_b2):
    def _A_ref(x, k_cache, v_cache, wq, wo):
        B, Sq, D = x.shape
        Hq, Dh = 16, 256
        Hkv, Sk = k_cache.shape[1], k_cache.shape[2]
        W = min(4096, max(256, Sk // 4))
        rep = Hq // Hkv
        q = (lambda x, *d: x.permute(*d))((x @ wq).reshape(B, Sq, Hq, Dh), 0, 2, 1, 3)
        k = (lambda x, n, axis: torch.repeat_interleave(x, n, dim=axis))(k_cache[:, :, Sk - W:, :], rep, axis=1)
        v = (lambda x, n, axis: torch.repeat_interleave(x, n, dim=axis))(v_cache[:, :, Sk - W:, :], rep, axis=1)
        scores = torch.einsum("bhqd,bhkd->bhqk", q, k) * (Dh ** -0.5)
        p = _softmax(scores, axis=-1)
        o = torch.einsum("bhqk,bhkd->bhqd", p, v)
        return (lambda x, *d: x.permute(*d))(o, 0, 2, 1, 3).reshape(B, Sq, Hq * Dh) @ wo

    def _B_ref(x, w1, b1, w2, b2):
        y = _layernorm(x, axis=-1)
        h = _gelu(torch.einsum("bsd,df->bsf", y, w1) + b1)
        return torch.einsum("bsf,fd->bsd", h, w2) + b2

    x = A_x
    xn = _layernorm(x, axis=-1)
    return x + _A_ref(xn, A_k_cache, A_v_cache, A_wq, A_wo) + _B_ref(xn, B_w1, B_b1, B_w2, B_b2)
```

```cpp
#include <hip/hip_runtime.h>
#include <hip/hip_bf16.h>
#include <stdint.h>

typedef unsigned short u16;
typedef unsigned v4u __attribute__((ext_vector_type(4)));
typedef __bf16 bf16x2 __attribute__((ext_vector_type(2)));

constexpr int D = 448, HQ = 16, HKV = 8, SK = 32768, DH = 256, W = 4096, F = 1792;
constexpr int NSPLIT = 32, KPS = W / NSPLIT;

__device__ __forceinline__ float bf2f(u16 v) { return __uint_as_float(((unsigned)v) << 16); }
__device__ __forceinline__ float lo2f(unsigned v) { return __uint_as_float(v << 16); }
__device__ __forceinline__ float hi2f(unsigned v) { return __uint_as_float(v & 0xffff0000u); }
__device__ __forceinline__ u16 f2bf(float f) { __hip_bfloat16 b = __float2bfloat16(f); return *reinterpret_cast<u16*>(&b); }
__device__ __forceinline__ float rbf(float f) { return f; }

__device__ __forceinline__ float wave_sum(float v) {
  for (int o = 32; o > 0; o >>= 1) v += __shfl_xor(v, o);
  return v;
}
__device__ __forceinline__ float wave_max(float v) {
  for (int o = 32; o > 0; o >>= 1) v = fmaxf(v, __shfl_xor(v, o));
  return v;
}

__device__ void compute_ln(const u16* x, float* s_xn, float* s_y, float* s_red) {
  int tid = threadIdx.x;
  int lane = tid & 63;
  float v[7];
  float s = 0.f;
  for (int i = 0; i < 7; ++i) { v[i] = bf2f(x[lane + 64 * i]); s += v[i]; }
  float m = wave_sum(s) * (1.f / D);
  float q = 0.f;
  for (int i = 0; i < 7; ++i) { float d = v[i] - m; q += d * d; }
  float var = wave_sum(q) * (1.f / D);
  float inv = 1.f / sqrtf(var + 1e-5f);
  float s2 = 0.f;
  for (int i = 0; i < 7; ++i) { v[i] = rbf((v[i] - m) * inv); s2 += v[i]; }
  float m2 = wave_sum(s2) * (1.f / D);
  float q2 = 0.f;
  for (int i = 0; i < 7; ++i) { float d = v[i] - m2; q2 += d * d; }
  float var2 = wave_sum(q2) * (1.f / D);
  float inv2 = 1.f / sqrtf(var2 + 1e-5f);
  if (tid < 64) {
    for (int i = 0; i < 7; ++i) { s_xn[lane + 64 * i] = v[i]; s_y[lane + 64 * i] = rbf((v[i] - m2) * inv2); }
  }
  __syncthreads();
}

__global__ void __launch_bounds__(256) k1(const u16* x, const u16* wq, const u16* w1, const u16* b1, float* qout, float* hout) {
  __shared__ float s_xn[D], s_y[D], s_red[32 * 64];
  compute_ln(x, s_xn, s_y, s_red);
  int b = blockIdx.x;
  bool isq = b < 64;
  const u16* Wm = isq ? wq : w1;
  int ld = isq ? 4096 : F;
  int c0 = (isq ? b : b - 64) * 64;
  const float* xin = isq ? s_xn : s_y;
  int tid = threadIdx.x;
  int cg = tid & 7, rg = tid >> 3;
  float acc[8] = {0, 0, 0, 0, 0, 0, 0, 0};
  for (int r = rg; r < D; r += 32) {
    v4u w = *reinterpret_cast<const v4u*>(Wm + (size_t)r * ld + c0 + cg * 8);
    float xv = xin[r];
    for (int k = 0; k < 4; ++k) { acc[2 * k] += xv * lo2f(w[k]); acc[2 * k + 1] += xv * hi2f(w[k]); }
  }
  for (int k = 0; k < 8; ++k) s_red[rg * 64 + cg * 8 + k] = acc[k];
  __syncthreads();
  if (tid < 64) {
    float s = 0.f;
    for (int r = 0; r < 32; ++r) s += s_red[r * 64 + tid];
    if (isq) qout[c0 + tid] = rbf(s);
    else {
      float z = rbf(rbf(s) + bf2f(b1[c0 + tid]));
      float u = 0.7978845608028654f * (z + 0.044715f * z * z * z);
      float g = 0.5f * z * (1.f + tanhf(u));
      hout[c0 + tid] = rbf(g);
    }
  }
}

__global__ void __launch_bounds__(256) k2(const u16* kc, const u16* vc, const float* q, float* po, float* pml) {
  __shared__ float s_q[2 * DH];
  __shared__ float s_s[2 * KPS];
  __shared__ float s_o[4 * 2 * DH];
  __shared__ float s_ml[4];
  int g = blockIdx.x & 7, s = blockIdx.x >> 3;
  int tid = threadIdx.x, lane = tid & 63, wave = tid >> 6;
  for (int i = tid; i < 512; i += 256) s_q[i] = q[g * 512 + i];
  __syncthreads();
  const u16* kb = kc + ((size_t)g * SK + (SK - W) + s * KPS) * DH;
  const u16* vb = vc + ((size_t)g * SK + (SK - W) + s * KPS) * DH;
  int c = lane & 31, half = lane >> 5;
  float q0[8], q1[8];
  for (int j = 0; j < 8; ++j) { q0[j] = s_q[c * 8 + j]; q1[j] = s_q[256 + c * 8 + j]; }
  for (int i = 0; i < 16; ++i) {
    int key = wave * 32 + i * 2 + half;
    v4u kv = *reinterpret_cast<const v4u*>(kb + (size_t)key * DH + c * 8);
    float a0 = 0.f, a1 = 0.f;
    for (int k = 0; k < 4; ++k) {
      float lo = lo2f(kv[k]), hi = hi2f(kv[k]);
      a0 += lo * q0[2 * k] + hi * q0[2 * k + 1];
      a1 += lo * q1[2 * k] + hi * q1[2 * k + 1];
    }
    for (int o = 16; o > 0; o >>= 1) { a0 += __shfl_xor(a0, o); a1 += __shfl_xor(a1, o); }
    if (c == 0) { s_s[key] = a0 * 0.0625f; s_s[KPS + key] = a1 * 0.0625f; }
  }
  __syncthreads();
  if (wave < 2) {
    float a = s_s[wave * KPS + lane], b2 = s_s[wave * KPS + 64 + lane];
    float m = wave_max(fmaxf(a, b2));
    float ea = __expf(a - m), eb = __expf(b2 - m);
    float l = wave_sum(ea + eb);
    s_s[wave * KPS + lane] = ea; s_s[wave * KPS + 64 + lane] = eb;
    if (lane == 0) { s_ml[wave * 2] = m; s_ml[wave * 2 + 1] = l; }
  }
  __syncthreads();
  float o0[8] = {0, 0, 0, 0, 0, 0, 0, 0}, o1[8] = {0, 0, 0, 0, 0, 0, 0, 0};
  for (int i = 0; i < 16; ++i) {
    int key = wave * 32 + i * 2 + half;
    v4u vv = *reinterpret_cast<const v4u*>(vb + (size_t)key * DH + c * 8);
    float p0 = s_s[key], p1 = s_s[KPS + key];
    for (int k = 0; k < 4; ++k) {
      float lo = lo2f(vv[k]), hi = hi2f(vv[k]);
      o0[2 * k] += p0 * lo; o0[2 * k + 1] += p0 * hi;
      o1[2 * k] += p1 * lo; o1[2 * k + 1] += p1 * hi;
    }
  }
  for (int j = 0; j < 8; ++j) { o0[j] += __shfl_xor(o0[j], 32); o1[j] += __shfl_xor(o1[j], 32); }
  if (half == 0) for (int j = 0; j < 8; ++j) { s_o[wave * 512 + c * 8 + j] = o0[j]; s_o[wave * 512 + 256 + c * 8 + j] = o1[j]; }
  __syncthreads();
  for (int i = tid; i < 512; i += 256) {
    float v = s_o[i] + s_o[512 + i] + s_o[1024 + i] + s_o[1536 + i];
    po[((size_t)g * NSPLIT + s) * 512 + i] = v;
  }
  if (tid < 4) pml[((size_t)g * NSPLIT + s) * 4 + tid] = s_ml[tid];
}

__global__ void __launch_bounds__(256) k3(const float* po, const float* pml, const float* h, const u16* wo, const u16* w2, float* part) {
  __shared__ float s_v[64];
  __shared__ float s_red[4 * D];
  int b = blockIdx.x, tid = threadIdx.x;
  bool iso = b < 64;
  if (tid < 64) {
    if (iso) {
      int col = b * 64 + tid;
      int head = col >> 8, g = head >> 1, hh = head & 1, dd = col & 255;
      float M = -INFINITY;
      for (int s = 0; s < NSPLIT; ++s) M = fmaxf(M, pml[((size_t)g * NSPLIT + s) * 4 + hh * 2]);
      float L = 0.f, o = 0.f;
      for (int s = 0; s < NSPLIT; ++s) {
        float w = __expf(pml[((size_t)g * NSPLIT + s) * 4 + hh * 2] - M);
        L += w * pml[((size_t)g * NSPLIT + s) * 4 + hh * 2 + 1];
        o += w * po[((size_t)g * NSPLIT + s) * 512 + hh * 256 + dd];
      }
      s_v[tid] = rbf(o / L);
    } else {
      s_v[tid] = h[(b - 64) * 64 + tid];
    }
  }
  __syncthreads();
  const u16* Wm = iso ? wo + (size_t)b * 64 * D : w2 + (size_t)(b - 64) * 64 * D;
  int lane = tid & 63, wave = tid >> 6;
  float acc[8] = {0, 0, 0, 0, 0, 0, 0, 0};
  if (lane < 56) {
    for (int r = wave; r < 64; r += 4) {
      v4u w = *reinterpret_cast<const v4u*>(Wm + (size_t)r * D + lane * 8);
      float xv = s_v[r];
      for (int k = 0; k < 4; ++k) { acc[2 * k] += xv * lo2f(w[k]); acc[2 * k + 1] += xv * hi2f(w[k]); }
    }
    for (int k = 0; k < 8; ++k) s_red[wave * D + lane * 8 + k] = acc[k];
  }
  __syncthreads();
  for (int i = tid; i < D; i += 256) part[(size_t)b * D + i] = s_red[i] + s_red[D + i] + s_red[2 * D + i] + s_red[3 * D + i];
}

__global__ void __launch_bounds__(448) k4(const float* part, const u16* x, const u16* b2, u16* out) {
  int c = threadIdx.x;
  float a = 0.f, m = 0.f;
  for (int b = 0; b < 64; ++b) a += part[(size_t)b * D + c];
  for (int b = 64; b < 92; ++b) m += part[(size_t)b * D + c];
  float r = rbf(bf2f(x[c]) + rbf(a));
  float mm = rbf(rbf(m) + bf2f(b2[c]));
  out[c] = f2bf(r + mm);
}

extern "C" void kernel_launch(void* const* d_in, const int* in_sizes, int n_in, void* d_out, int out_size, void* d_ws, size_t ws_size, hipStream_t stream) {
  const u16* x = (const u16*)d_in[0];
  const u16* kc = (const u16*)d_in[1];
  const u16* vc = (const u16*)d_in[2];
  const u16* wq = (const u16*)d_in[3];
  const u16* wo = (const u16*)d_in[4];
  const u16* w1 = (const u16*)d_in[6];
  const u16* b1 = (const u16*)d_in[7];
  const u16* w2 = (const u16*)d_in[8];
  const u16* b2 = (const u16*)d_in[9];
  float* ws = (float*)d_ws;
  float* q = ws;
  float* h = ws + 4096;
  float* pml = ws + 8192;
  float* po = ws + 16384;
  float* part = ws + 16384 + 131072;
  k1<<<64 + 28, 256, 0, stream>>>(x, wq, w1, b1, q, h);
  k2<<<256, 256, 0, stream>>>(kc, vc, q, po, pml);
  k3<<<92, 256, 0, stream>>>(po, pml, h, wo, w2, part);
  k4<<<1, 448, 0, stream>>>(part, x, b2, (u16*)d_out);
}
```

```cpp
#include <hip/hip_runtime.h>
#include <hip/hip_bf16.h>
#include <stdint.h>

#define GAS __attribute__((address_space(1)))
#define LAS __attribute__((address_space(3)))
typedef unsigned short u16;
typedef unsigned v4u __attribute__((ext_vector_type(4)));
typedef unsigned v2u __attribute__((ext_vector_type(2)));
typedef float v4f __attribute__((ext_vector_type(4)));
typedef float v2f __attribute__((ext_vector_type(2)));
typedef __bf16 bf16x2 __attribute__((ext_vector_type(2)));
typedef GAS unsigned gu32;
typedef GAS unsigned long long gu64;
#define RLX_AGENT __ATOMIC_RELAXED, __HIP_MEMORY_SCOPE_AGENT
#define SB() __builtin_amdgcn_sched_barrier(0)
#define CBAR() asm volatile("" ::: "memory")

constexpr int D = 448, SK = 32768, DH = 256, WIN = 4096, F = 1792;
constexpr float L2E = 1.4426950408889634f;
constexpr unsigned SPIN_CAP = 1u << 22;

constexpr unsigned WS_EPOCH = 0;
constexpr unsigned WS_QG = 4096;
constexpr unsigned WS_SG = WS_QG + 16384;
constexpr unsigned WS_OG = 65536;
constexpr unsigned WS_FG = WS_OG + (1u << 20);
constexpr unsigned WS_END = WS_FG + 256u * 448u * 8u;
constexpr unsigned WS_DBG = 4u << 20;

constexpr int L_FLAG = 0;
constexpr int L_Q = 16;
constexpr int L_P = L_Q + 256;
constexpr int L_WMAX = L_P + 8 * 36;
constexpr int L_MLP = L_WMAX + 8;
constexpr int L_END = L_MLP + 448;

#define STAMP(i) do {} while (0)
#define STAMP_DECL
#define STAMP_FLUSH() do {} while (0)

__device__ __forceinline__ float u2f(unsigned v) { return __builtin_bit_cast(float, v); }
__device__ __forceinline__ unsigned f2u(float v) { return __builtin_bit_cast(unsigned, v); }
__device__ __forceinline__ float lo2f(unsigned v) { return u2f(v << 16); }
__device__ __forceinline__ float hi2f(unsigned v) { return u2f(v & 0xffff0000u); }
__device__ __forceinline__ unsigned pack_bf16(float lo, float hi) {
  bf16x2 r; r[0] = (__bf16)lo; r[1] = (__bf16)hi; return __builtin_bit_cast(unsigned, r);
}
__device__ __forceinline__ float rbf(float f) { return (float)(__bf16)f; }
__device__ __forceinline__ float dot2(unsigned a, unsigned b, float c) {
  return __builtin_amdgcn_fdot2_f32_bf16(__builtin_bit_cast(bf16x2, a), __builtin_bit_cast(bf16x2, b), c, false);
}
template <int CTRL> __device__ __forceinline__ float dpp(float x) {
  return u2f((unsigned)__builtin_amdgcn_mov_dpp((int)f2u(x), CTRL, 0xf, 0xf, true));
}
constexpr int ROR8 = 0x128, ROR4 = 0x124, ROR2 = 0x122, ROR1 = 0x121, XOR7 = 0x141, XOR2 = 0x4E, XOR1 = 0xB1;
__device__ __forceinline__ float tr32(float a, float b) {
  auto r = __builtin_amdgcn_permlane32_swap(f2u(a), f2u(b), false, false); return u2f(r[0]) + u2f(r[1]);
}
__device__ __forceinline__ float tr16(float a, float b) {
  auto r = __builtin_amdgcn_permlane16_swap(f2u(a), f2u(b), false, false); return u2f(r[0]) + u2f(r[1]);
}
__device__ __forceinline__ float rowsum(float v) { v += dpp<ROR8>(v); v += dpp<ROR4>(v); v += dpp<ROR2>(v); v += dpp<ROR1>(v); return v; }
__device__ __forceinline__ float rowmax(float v) { v = fmaxf(v, dpp<ROR8>(v)); v = fmaxf(v, dpp<ROR4>(v)); v = fmaxf(v, dpp<ROR2>(v)); v = fmaxf(v, dpp<ROR1>(v)); return v; }
__device__ __forceinline__ float allsum(float v) { v = rowsum(v); v = tr16(v, v); v = tr32(v, v); return v; }
__device__ __forceinline__ float max32(float v) {
  auto r = __builtin_amdgcn_permlane32_swap(f2u(v), f2u(v), false, false); return fmaxf(u2f(r[0]), u2f(r[1]));
}
template <int CTRL> __device__ __forceinline__ float trrow(float a, float b, bool bit) {
  float keep = bit ? b : a, send = bit ? a : b;
  return keep + dpp<CTRL>(send);
}
__device__ __forceinline__ v4u ntload(const u16* p) { return __builtin_nontemporal_load(reinterpret_cast<const v4u*>(p)); }

__device__ __forceinline__ void layernorm7(float (&v)[7]) {
  float s = 0.f;
#pragma unroll
  for (int i = 0; i < 7; ++i) s += v[i];
  const float mean = allsum(s) * (1.f / D);
  float q = 0.f;
#pragma unroll
  for (int i = 0; i < 7; ++i) { const float d = v[i] - mean; q = fmaf(d, d, q); }
  const float var = rbf(allsum(q) * (1.f / D));
  const float den = rbf(sqrtf(rbf(var + 1e-5f)));
  const float m = rbf(mean);
#pragma unroll
  for (int i = 0; i < 7; ++i) v[i] = rbf(rbf(v[i] - m) / den);
}

__device__ __forceinline__ void gemv8(const float (&xv)[7], const v4u (&w)[7], float& c0, float& c1) {
  float acc[8];
#pragma unroll
  for (int k = 0; k < 8; ++k) acc[k] = 0.f;
#pragma unroll
  for (int i = 0; i < 7; ++i)
#pragma unroll
    for (int k = 0; k < 4; ++k) { acc[2 * k] = fmaf(xv[i], lo2f(w[i][k]), acc[2 * k]); acc[2 * k + 1] = fmaf(xv[i], hi2f(w[i][k]), acc[2 * k + 1]); }
  float a[4];
#pragma unroll
  for (int k = 0; k < 4; ++k) a[k] = tr32(acc[k], acc[k + 4]);
  c0 = rowsum(tr16(a[0], a[2]));
  c1 = rowsum(tr16(a[1], a[3]));
}

__device__ __forceinline__ void lds_wait_eq(volatile LAS unsigned* p, unsigned want) {
#pragma unroll 1
  for (unsigned n = 0; n < SPIN_CAP; ++n) { if (*p == want) break; __builtin_amdgcn_s_sleep(1); }
  CBAR();
}
__device__ __forceinline__ void lds_wait4(volatile LAS v4u* p) {
#pragma unroll 1
  for (unsigned n = 0; n < SPIN_CAP; ++n) { v4u f = *p; if ((f[0] & f[1] & f[2] & f[3]) == 1u) break; __builtin_amdgcn_s_sleep(1); }
  CBAR();
}

__global__ void __launch_bounds__(768, 3)
fused_decode(const u16* __restrict__ x, const u16* __restrict__ kc, const u16* __restrict__ vc, const u16* __restrict__ wq,
             const u16* __restrict__ wo, const u16* __restrict__ w1, const u16* __restrict__ b1, const u16* __restrict__ w2,
             const u16* __restrict__ b2, u16* __restrict__ out, unsigned char* ws) {
  __shared__ __attribute__((aligned(16))) unsigned lds_[L_END];
  LAS unsigned* lds = (LAS unsigned*)lds_;
  volatile LAS unsigned* flag = (volatile LAS unsigned*)lds;
  const int tid = threadIdx.x, lane = tid & 63, wave = __builtin_amdgcn_readfirstlane(tid >> 6);
  const int b = blockIdx.x, g = b & 7, s = b >> 3;
  const int ell = (s >> 3) * 8 + g;
  const bool has_mlp = ell < 28;
  const auto rs = __builtin_amdgcn_make_buffer_rsrc(ws, 0, WS_END, 0x00020000);
  STAMP_DECL;
  STAMP(0);

  if (wave < 4) {
    __builtin_amdgcn_s_setprio(3);
    if (wave < 2) {
      const unsigned tag = __hip_atomic_load((gu32*)(ws + WS_EPOCH), RLX_AGENT) + 1u;
      float xv[7]; v4u w[7];
#pragma unroll
      for (int i = 0; i < 7; ++i) xv[i] = lo2f(x[64 * i + lane]);
      const u16* wp = wq + (size_t)lane * 4096 + g * 512 + s * 16 + wave * 8;
#pragma unroll
      for (int i = 0; i < 7; ++i) { w[i] = ntload(wp + (size_t)i * 64 * 4096); SB(); }
      SB();
      __builtin_amdgcn_s_barrier();
      layernorm7(xv);
      STAMP(1);
      float c0, c1;
      gemv8(xv, w, c0, c1);
      const unsigned pk = pack_bf16(c0, c1);
      if ((lane & 15) == 0)
        __hip_atomic_store((gu64*)(ws + WS_QG) + g * 256 + s * 8 + wave * 4 + (lane >> 4), ((unsigned long long)tag << 32) | pk, RLX_AGENT);
      CBAR();
      flag[2 + wave] = 1u;
      STAMP(2);
    } else if (wave == 2) {
      float xv[7]; v4u w[7];
      const int c0i = (has_mlp ? ell : 0) * 64 + (s & 7) * 8;
#pragma unroll
      for (int i = 0; i < 7; ++i) xv[i] = lo2f(x[64 * i + lane]);
      const unsigned bb = *reinterpret_cast<const unsigned*>(b1 + c0i + 2 * (lane >> 4));
      const u16* wp = w1 + (size_t)lane * F + c0i;
      if (has_mlp) {
#pragma unroll
        for (int i = 0; i < 7; ++i) { w[i] = ntload(wp + (size_t)i * 64 * F); SB(); }
      }
      SB();
      __builtin_amdgcn_s_barrier();
      float acc[8];
#pragma unroll
      for (int k = 0; k < 8; ++k) acc[k] = 0.f;
      if (has_mlp) {
        layernorm7(xv);
        layernorm7(xv);
        float z0, z1;
        gemv8(xv, w, z0, z1);
        z0 = rbf(rbf(z0) + lo2f(bb));
        z1 = rbf(rbf(z1) + hi2f(bb));
        const float u0 = 0.7978845608028654f * (z0 + 0.044715f * z0 * z0 * z0);
        const float u1 = 0.7978845608028654f * (z1 + 0.044715f * z1 * z1 * z1);
        const float t0 = 1.f - 2.f / (__builtin_amdgcn_exp2f(2.f * L2E * u0) + 1.f);
        const float t1 = 1.f - 2.f / (__builtin_amdgcn_exp2f(2.f * L2E * u1) + 1.f);
        const float h0 = rbf(0.5f * z0 * (1.f + t0)), h1 = rbf(0.5f * z1 * (1.f + t1));
        STAMP(1);
        __builtin_amdgcn_s_setprio(0);
        v4u r2[8];
        const u16* w2p = w2 + (size_t)c0i * D + (lane < 56 ? lane : 55) * 8;
#pragma unroll
        for (int r = 0; r < 8; ++r) { r2[r] = ntload(w2p + r * D); SB(); }
        SB();
#pragma unroll
        for (int r = 0; r < 8; ++r) {
          const float hr = u2f(__builtin_amdgcn_readlane(f2u((r & 1) ? h1 : h0), (r >> 1) * 16));
#pragma unroll
          for (int k = 0; k < 4; ++k) { acc[2 * k] = fmaf(hr, lo2f(r2[r][k]), acc[2 * k]); acc[2 * k + 1] = fmaf(hr, hi2f(r2[r][k]), acc[2 * k + 1]); }
        }
      }
      if (lane < 56) {
        LAS v4f* mp = (LAS v4f*)(lds + L_MLP + lane * 8);
        mp[0] = v4f{acc[0], acc[1], acc[2], acc[3]};
        mp[1] = v4f{acc[4], acc[5], acc[6], acc[7]};
      }
      CBAR();
      flag[1] = 1u;
      STAMP(2);
    } else {
      const unsigned tag = __hip_atomic_load((gu32*)(ws + WS_EPOCH), RLX_AGENT) + 1u;
      const int cp = (has_mlp ? ell : 0) * 8 + (s & 7);
      const unsigned xr = *reinterpret_cast<const unsigned*>(x + 2 * cp);
      const unsigned br = *reinterpret_cast<const unsigned*>(b2 + 2 * cp);
      if (lane < 16) flag[lane] = 0u;
      asm volatile("s_waitcnt lgkmcnt(0)" ::: "memory");
      __builtin_amdgcn_s_barrier();
      lds_wait_eq(flag + 2, 1u);
      lds_wait_eq(flag + 3, 1u);
      STAMP(1);
#define QDELAY 8
      for (int i = 0; i < QDELAY; ++i) __builtin_amdgcn_s_sleep(4);
      const unsigned qoff = WS_QG + g * 2048 + lane * 32;
      v4u a0, a1;
      for (unsigned n = 0; n < SPIN_CAP; ++n) {
        CBAR();
        a0 = __builtin_amdgcn_raw_buffer_load_b128(rs, qoff, 0, 16);
        a1 = __builtin_amdgcn_raw_buffer_load_b128(rs, qoff + 16, 0, 16);
        const bool ok = (a0[1] == tag) & (a0[3] == tag) & (a1[1] == tag) & (a1[3] == tag);
        if (__all(ok)) break;
      }
      *(LAS v4u*)(lds + L_Q + lane * 4) = v4u{a0[0], a0[2], a1[0], a1[2]};
      CBAR();
      flag[0] = 1u;
      STAMP(2);
      if (has_mlp) {
        lds_wait4((volatile LAS v4u*)(flag + 12));
        STAMP(3);
        const unsigned foff = WS_FG + lane * 3584 + cp * 16;
        v4u f0, f1, f2, f3;
        for (unsigned n = 0; n < SPIN_CAP; ++n) {
          CBAR();
          f0 = __builtin_amdgcn_raw_buffer_load_b128(rs, foff, 0, 16);
          f1 = __builtin_amdgcn_raw_buffer_load_b128(rs, foff + 64 * 3584, 0, 16);
          f2 = __builtin_amdgcn_raw_buffer_load_b128(rs, foff + 128 * 3584, 0, 16);
          f3 = __builtin_amdgcn_raw_buffer_load_b128(rs, foff + 192 * 3584, 0, 16);
          const bool ok = (f0[1] == tag) & (f0[3] == tag) & (f1[1] == tag) & (f1[3] == tag) & (f2[1] == tag) & (f2[3] == tag) & (f3[1] == tag) & (f3[3] == tag);
          if (__all(ok)) break;
        }
        STAMP(4);
        float s0 = (u2f(f0[0]) + u2f(f1[0])) + (u2f(f2[0]) + u2f(f3[0]));
        float s1 = (u2f(f0[2]) + u2f(f1[2])) + (u2f(f2[2]) + u2f(f3[2]));
        s0 = allsum(s0); s1 = allsum(s1);
        if (lane == 0) {
          const float o0 = lo2f(xr) + s0 + lo2f(br), o1 = hi2f(xr) + s1 + hi2f(br);
          *reinterpret_cast<unsigned*>(out + 2 * cp) = pack_bf16(o0, o1);
          if (b == 0) __hip_atomic_store((gu32*)(ws + WS_EPOCH), tag, RLX_AGENT);
        }
        STAMP(5);
      }
    }
  } else if (wave < 8) {
    const int kw = wave - 4;
    __builtin_amdgcn_s_setprio(2);
    const unsigned tag = __hip_atomic_load((gu32*)(ws + WS_EPOCH), RLX_AGENT) + 1u;
    const u16* kp = kc + ((size_t)g * SK + (SK - WIN) + s * 128 + kw * 32) * DH + lane * 8;
    v4u kr[16];
    kr[0] = ntload(kp);
    kr[1] = ntload(kp + 512);
    SB();
    __builtin_amdgcn_s_barrier();
    SB();
#pragma unroll
    for (int i = 2; i < 16; ++i) { kr[i] = ntload(kp + i * 512); SB(); }
    SB();
    STAMP(1);
    lds_wait_eq(flag, 1u);
    STAMP(2);
    const v4u q0 = *(const LAS v4u*)(lds + L_Q + (lane & 31) * 4);
    const v4u q1 = *(const LAS v4u*)(lds + L_Q + 128 + (lane & 31) * 4);
    float y[16];
#pragma unroll
    for (int i = 0; i < 16; ++i) {
      float p0 = 0.f, p1 = 0.f;
#pragma unroll
      for (int k = 0; k < 4; ++k) { p0 = dot2(kr[i][k], q0[k], p0); p1 = dot2(kr[i][k], q1[k], p1); }
      y[i] = tr16(p0, p1);
    }
    const bool b3 = lane & 8, b2_ = lane & 4, b1_ = lane & 2, b0_ = lane & 1;
#pragma unroll
    for (int i = 0; i < 8; ++i) y[i] = trrow<ROR8>(y[i], y[i + 8], b3);
#pragma unroll
    for (int i = 0; i < 4; ++i) y[i] = trrow<XOR7>(y[i], y[i + 4], b2_);
#pragma unroll
    for (int i = 0; i < 2; ++i) y[i] = trrow<XOR2>(y[i], y[i + 2], b1_);
    const float sc = rbf(trrow<XOR1>(y[0], y[1], b0_)) * 0.0625f;
    const float m = max32(rowmax(sc));
    const float p = __builtin_amdgcn_exp2f((sc - m) * L2E);
    {
      const int i = lane & 15, h = (lane >> 4) & 1, half = lane >> 5;
      const int ks = (2 * i + half) & 7, ii = 4 * kw + (i >> 2);
      lds[L_P + ks * 36 + ii * 2 + h] = f2u(p);
      if ((lane & 47) == 0) lds[L_WMAX + kw * 2 + h] = f2u(m);
    }
    CBAR();
    flag[4 + kw] = 1u;
    STAMP(3);
    __builtin_amdgcn_s_setprio(0);
    const int r4 = lane >> 4, c16 = lane & 15;
    const u16* wop = wo + (size_t)(g * 512 + s * 16 + 4 * r4) * D + kw * 112 + (c16 < 14 ? c16 : 13) * 8;
    v4u wr[4];
#pragma unroll
    for (int i = 0; i < 4; ++i) { wr[i] = ntload(wop + i * D); SB(); }
    SB();
    __builtin_amdgcn_s_setprio(3);
    lds_wait_eq(flag + 1, 1u);
    const int col = kw * 112 + (c16 < 14 ? c16 : 13) * 8 + 2 * r4;
    const v2f mlp = *(const LAS v2f*)(lds + L_MLP + col);
    lds_wait4((volatile LAS v4u*)(flag + 8));
    STAMP(4);
    const unsigned ooff = WS_OG + ((g * 32 + s) * 32 + c16) * 128 + r4 * 32;
    const unsigned soff = WS_SG + ((g * 2 + (s >> 4)) * 32 + c16) * 16;
    v4u oa0, oa1, ob0, ob1, sa, sb;
    for (unsigned n = 0; n < SPIN_CAP; ++n) {
      CBAR();
      sa = __builtin_amdgcn_raw_buffer_load_b128(rs, soff, 0, 16);
      sb = __builtin_amdgcn_raw_buffer_load_b128(rs, soff + 256, 0, 16);
      oa0 = __builtin_amdgcn_raw_buffer_load_b128(rs, ooff, 0, 16);
      oa1 = __builtin_amdgcn_raw_buffer_load_b128(rs, ooff + 16, 0, 16);
      ob0 = __builtin_amdgcn_raw_buffer_load_b128(rs, ooff + 2048, 0, 16);
      ob1 = __builtin_amdgcn_raw_buffer_load_b128(rs, ooff + 2048 + 16, 0, 16);
      const bool ok = (sa[1] == tag) & (sa[3] == tag) & (sb[1] == tag) & (sb[3] == tag) & (oa0[1] == tag) & (oa0[3] == tag) &
                      (oa1[1] == tag) & (oa1[3] == tag) & (ob0[1] == tag) & (ob0[3] == tag) & (ob1[1] == tag) & (ob1[3] == tag);
      if (__all(ok)) break;
    }
    STAMP(5);
    const float mA = u2f(sa[0]), lA = u2f(sa[2]), mB = u2f(sb[0]), lB = u2f(sb[2]);
    const float M = rowmax(fmaxf(mA, mB));
    const float eA = __builtin_amdgcn_exp2f((mA - M) * L2E), eB = __builtin_amdgcn_exp2f((mB - M) * L2E);
    const float den = rowsum(fmaf(eA, lA, eB * lB));
    float o[4];
    o[0] = rowsum(fmaf(eA, u2f(oa0[0]), eB * u2f(ob0[0])));
    o[1] = rowsum(fmaf(eA, u2f(oa0[2]), eB * u2f(ob0[2])));
    o[2] = rowsum(fmaf(eA, u2f(oa1[0]), eB * u2f(ob1[0])));
    o[3] = rowsum(fmaf(eA, u2f(oa1[2]), eB * u2f(ob1[2])));
    const float inv = __builtin_amdgcn_rcpf(den);
    float acc[8];
#pragma unroll
    for (int k = 0; k < 8; ++k) acc[k] = 0.f;
#pragma unroll
    for (int i = 0; i < 4; ++i) {
      const float oi = rbf(o[i] * inv);
#pragma unroll
      for (int k = 0; k < 4; ++k) { acc[2 * k] = fmaf(oi, lo2f(wr[i][k]), acc[2 * k]); acc[2 * k + 1] = fmaf(oi, hi2f(wr[i][k]), acc[2 * k + 1]); }
    }
    float a[4];
#pragma unroll
    for (int k = 0; k < 4; ++k) a[k] = tr32(acc[k], acc[k + 4]);
    const float r0 = tr16(a[0], a[2]) + mlp[0], r1 = tr16(a[1], a[3]) + mlp[1];
    if (c16 < 14)
      __builtin_amdgcn_raw_buffer_store_b128(v4u{f2u(r0), tag, f2u(r1), tag}, rs, WS_FG + b * 3584 + col * 8, 0, 16);
    CBAR();
    flag[12 + kw] = 1u;
    STAMP(6);
  } else {
    const int vw = wave - 8;
    __builtin_amdgcn_s_setprio(1);
    const unsigned tag = __hip_atomic_load((gu32*)(ws + WS_EPOCH), RLX_AGENT) + 1u;
    const int ks = lane >> 3, c = lane & 7;
    const u16* vp = vc + ((size_t)g * SK + (SK - WIN) + s * 128 + ks) * DH + vw * 64 + c * 8;
    v4u vr[16];
    __builtin_amdgcn_s_barrier();
    SB();
#pragma unroll
    for (int i = 0; i < 16; ++i) { vr[i] = ntload(vp + i * 8 * DH); SB(); }
    SB();
    STAMP(1);
    lds_wait4((volatile LAS v4u*)(flag + 4));
    STAMP(2);
    const v4f wm0 = *(const LAS v4f*)(lds + L_WMAX), wm1 = *(const LAS v4f*)(lds + L_WMAX + 4);
    const float M0 = fmaxf(fmaxf(wm0[0], wm0[2]), fmaxf(wm1[0], wm1[2]));
    const float M1 = fmaxf(fmaxf(wm0[1], wm0[3]), fmaxf(wm1[1], wm1[3]));
    float f0[4], f1[4];
    f0[0] = __builtin_amdgcn_exp2f((wm0[0] - M0) * L2E); f1[0] = __builtin_amdgcn_exp2f((wm0[1] - M1) * L2E);
    f0[1] = __builtin_amdgcn_exp2f((wm0[2] - M0) * L2E); f1[1] = __builtin_amdgcn_exp2f((wm0[3] - M1) * L2E);
    f0[2] = __builtin_amdgcn_exp2f((wm1[0] - M0) * L2E); f1[2] = __builtin_amdgcn_exp2f((wm1[1] - M1) * L2E);
    f0[3] = __builtin_amdgcn_exp2f((wm1[2] - M0) * L2E); f1[3] = __builtin_amdgcn_exp2f((wm1[3] - M1) * L2E);
    float p0[16], p1[16];
#pragma unroll
    for (int u = 0; u < 8; ++u) {
      const v4f t = *(const LAS v4f*)(lds + L_P + ks * 36 + u * 4);
      p0[2 * u] = t[0] * f0[u >> 1]; p1[2 * u] = t[1] * f1[u >> 1];
      p0[2 * u + 1] = t[2] * f0[u >> 1]; p1[2 * u + 1] = t[3] * f1[u >> 1];
    }
    float lsum;
    {
      float l0 = 0.f, l1 = 0.f;
#pragma unroll
      for (int i = 0; i < 16; ++i) { l0 += p0[i]; l1 += p1[i]; }
      lsum = tr32(l0, l1);
      lsum = tr16(lsum, lsum);
      lsum += dpp<ROR8>(lsum);
    }
    SB();
    float a0[8], a1[8];
#pragma unroll
    for (int k = 0; k < 8; ++k) { a0[k] = 0.f; a1[k] = 0.f; }
#pragma unroll
    for (int i = 0; i < 16; ++i) {
#pragma unroll
      for (int k = 0; k < 4; ++k) {
        const float lo = lo2f(vr[i][k]), hi = hi2f(vr[i][k]);
        a0[2 * k] = fmaf(p0[i], lo, a0[2 * k]); a0[2 * k + 1] = fmaf(p0[i], hi, a0[2 * k + 1]);
        a1[2 * k] = fmaf(p1[i], lo, a1[2 * k]); a1[2 * k + 1] = fmaf(p1[i], hi, a1[2 * k + 1]);
      }
      SB();
    }
    STAMP(3);
    float t8[8], t4[4];
#pragma unroll
    for (int k = 0; k < 8; ++k) t8[k] = tr32(a0[k], a1[k]);
#pragma unroll
    for (int k = 0; k < 4; ++k) t4[k] = tr16(t8[k], t8[k + 4]);
    const bool b3 = lane & 8;
    const float r0 = trrow<ROR8>(t4[0], t4[2], b3), r1 = trrow<ROR8>(t4[1], t4[3], b3);
    {
      const int h = lane >> 5, d = 4 * ((lane >> 4) & 1) + 2 * ((lane >> 3) & 1);
      const int j = h * 16 + 4 * vw + (c >> 1), colj = (c & 1) * 8 + d;
      __builtin_amdgcn_raw_buffer_store_b128(v4u{f2u(r0), tag, f2u(r1), tag}, rs, WS_OG + ((g * 32 + j) * 32 + s) * 128 + colj * 8, 0, 16);
      if (vw == 0 && (lane & 31) == 0)
        __builtin_amdgcn_raw_buffer_store_b128(v4u{f2u(h ? M1 : M0), tag, f2u(lsum), tag}, rs, WS_SG + ((g * 2 + h) * 32 + s) * 16, 0, 16);
    }
    CBAR();
    flag[8 + vw] = 1u;
    STAMP(4);
  }
  STAMP_FLUSH();
}

extern "C" void kernel_launch(void* const* d_in, const int* in_sizes, int n_in, void* d_out, int out_size, void* d_ws, size_t ws_size,
                              hipStream_t stream) {
  fused_decode<<<256, 768, 0, stream>>>((const u16*)d_in[0], (const u16*)d_in[1], (const u16*)d_in[2], (const u16*)d_in[3],
                                        (const u16*)d_in[4], (const u16*)d_in[6], (const u16*)d_in[7], (const u16*)d_in[8],
                                        (const u16*)d_in[9], (u16*)d_out, (unsigned char*)d_ws);
}
```

```cpp
#include <hip/hip_runtime.h>
#include <hip/hip_bf16.h>
#include <stdint.h>

#define GAS __attribute__((address_space(1)))
#define LAS __attribute__((address_space(3)))
typedef unsigned short u16;
typedef unsigned v4u __attribute__((ext_vector_type(4)));
typedef unsigned v2u __attribute__((ext_vector_type(2)));
typedef float v4f __attribute__((ext_vector_type(4)));
typedef float v2f __attribute__((ext_vector_type(2)));
typedef __bf16 bf16x2 __attribute__((ext_vector_type(2)));
typedef __bf16 bf16x8 __attribute__((ext_vector_type(8)));
typedef GAS unsigned gu32;
#define RLX_AGENT __ATOMIC_RELAXED, __HIP_MEMORY_SCOPE_AGENT
#define SB() __builtin_amdgcn_sched_barrier(0)
#define CBAR() asm volatile("" ::: "memory")
#define VMCNT(n) asm volatile("s_waitcnt vmcnt(%0)" ::"i"(n) : "memory")

#define WIN_K 4
#define WIN_V 2
#define QDEAD 300
#define QMIN 50
#define ODELAY 60
#define FDELAY 50
#define CODEPF 15360

constexpr int D = 448, SK = 32768, DH = 256, WIN = 4096, F = 1792;
constexpr float L2E = 1.4426950408889634f;
constexpr unsigned SPIN_G = 1u << 17;
constexpr unsigned SPIN_L = 1u << 21;

constexpr unsigned WS_EPOCH = 0;
constexpr unsigned WS_QG = 4096;
constexpr unsigned WS_SG = WS_QG + 65536;
constexpr unsigned WS_ZG = WS_SG + 32768;
constexpr unsigned WS_OG = 262144;
constexpr unsigned WS_FG = WS_OG + (512u << 10);
constexpr unsigned WS_END = WS_FG + 256u * 448u * 8u;

constexpr int L_Q = 32;
constexpr int L_P = L_Q + 256;
constexpr int L_WST = L_P + 16 * 20;
constexpr int L_QP = L_WST + 16;
constexpr int L_O = L_QP + 32;
constexpr int L_MLP = L_O + 16;
constexpr int L_END = L_MLP + 448;

__device__ __forceinline__ float u2f(unsigned v) { return __builtin_bit_cast(float, v); }
__device__ __forceinline__ unsigned f2u(float v) { return __builtin_bit_cast(unsigned, v); }
__device__ __forceinline__ float lo2f(unsigned v) { return u2f(v << 16); }
__device__ __forceinline__ float hi2f(unsigned v) { return u2f(v & 0xffff0000u); }
__device__ __forceinline__ unsigned pack_bf16(float lo, float hi) {
  bf16x2 r; r[0] = (__bf16)lo; r[1] = (__bf16)hi; return __builtin_bit_cast(unsigned, r);
}
__device__ __forceinline__ float rbf(float f) { return (float)(__bf16)f; }
__device__ __forceinline__ float dot2(unsigned a, unsigned b, float c) {
  return __builtin_amdgcn_fdot2_f32_bf16(__builtin_bit_cast(bf16x2, a), __builtin_bit_cast(bf16x2, b), c, false);
}
__device__ __forceinline__ v2f pkfma(v2f a, v2f b, v2f c) { return __builtin_elementwise_fma(a, b, c); }
template <int CTRL> __device__ __forceinline__ float dpp(float x) {
  return u2f((unsigned)__builtin_amdgcn_mov_dpp((int)f2u(x), CTRL, 0xf, 0xf, true));
}
constexpr int ROR8 = 0x128, ROR4 = 0x124, ROR2 = 0x122, ROR1 = 0x121, XOR7 = 0x141, XOR2 = 0x4E, XOR1 = 0xB1;
__device__ __forceinline__ float tr32(float a, float b) {
  auto r = __builtin_amdgcn_permlane32_swap(f2u(a), f2u(b), false, false); return u2f(r[0]) + u2f(r[1]);
}
__device__ __forceinline__ float tr16(float a, float b) {
  auto r = __builtin_amdgcn_permlane16_swap(f2u(a), f2u(b), false, false); return u2f(r[0]) + u2f(r[1]);
}
__device__ __forceinline__ float rowsum(float v) { v += dpp<ROR8>(v); v += dpp<ROR4>(v); v += dpp<ROR2>(v); v += dpp<ROR1>(v); return v; }
__device__ __forceinline__ float rowmax(float v) {
  v = fmaxf(v, dpp<ROR8>(v)); v = fmaxf(v, dpp<ROR4>(v)); v = fmaxf(v, dpp<ROR2>(v)); v = fmaxf(v, dpp<ROR1>(v)); return v;
}
__device__ __forceinline__ float max16(float v) {
  auto r = __builtin_amdgcn_permlane16_swap(f2u(v), f2u(v), false, false); return fmaxf(u2f(r[0]), u2f(r[1]));
}
__device__ __forceinline__ float max32(float v) {
  auto r = __builtin_amdgcn_permlane32_swap(f2u(v), f2u(v), false, false); return fmaxf(u2f(r[0]), u2f(r[1]));
}
template <int CTRL> __device__ __forceinline__ float trrow(float a, float b, bool bit) {
  const float keep = bit ? b : a, send = bit ? a : b;
  return keep + dpp<CTRL>(send);
}
#define MKRS(p, bytes) __builtin_amdgcn_make_buffer_rsrc((void*)(p), 0, (bytes), 0x00020000)
#define NTLD(rsrc, voff, soff) __builtin_amdgcn_raw_buffer_load_b128((rsrc), (voff), (soff), 2)

__device__ __forceinline__ void ln_stats8(const float (&v)[8], bool act, float& m, float& inv) {
  const float c = u2f(__builtin_amdgcn_readfirstlane(f2u(v[0])));
  float d[8];
#pragma unroll
  for (int i = 0; i < 8; ++i) d[i] = v[i] - c;
  float s = ((d[0] + d[1]) + (d[2] + d[3])) + ((d[4] + d[5]) + (d[6] + d[7]));
  float q = fmaf(d[0], d[0], fmaf(d[1], d[1], fmaf(d[2], d[2], d[3] * d[3]))) + fmaf(d[4], d[4], fmaf(d[5], d[5], fmaf(d[6], d[6], d[7] * d[7])));
  s = act ? s : 0.f; q = act ? q : 0.f;
  float t = tr32(s, q);
  t = tr16(t, t);
  t = rowsum(t);
  const float S = u2f(__builtin_amdgcn_readlane(f2u(t), 0)) * (1.f / D), Q = u2f(__builtin_amdgcn_readlane(f2u(t), 32)) * (1.f / D);
  const float var = rbf(fmaxf(fmaf(-S, S, Q), 0.f));
  inv = 1.f / rbf(sqrtf(rbf(var + 1e-5f)));
  m = rbf(c + S);
}
__device__ __forceinline__ float ln_apply(float v, float m, float inv) { return rbf(rbf(v - m) * inv); }

__device__ __forceinline__ void gemv32(const float (&xv)[7], const v4u (&w)[7], float& c0, float& c1) {
  v2f acc[4];
#pragma unroll
  for (int k = 0; k < 4; ++k) acc[k] = v2f{0.f, 0.f};
#pragma unroll
  for (int i = 0; i < 7; ++i) {
#pragma unroll
    for (int k = 0; k < 4; ++k) acc[k] = pkfma(v2f{lo2f(w[i][k]), hi2f(w[i][k])}, v2f{xv[i], xv[i]}, acc[k]);
    SB();
  }
  const float a0 = tr32(acc[0][0], acc[2][0]), a1 = tr32(acc[0][1], acc[2][1]);
  const float a2 = tr32(acc[1][0], acc[3][0]), a3 = tr32(acc[1][1], acc[3][1]);
  c0 = tr16(a0, a2); c1 = tr16(a1, a3);
  c0 += dpp<ROR8>(c0); c1 += dpp<ROR8>(c1);
  c0 += dpp<ROR4>(c0); c1 += dpp<ROR4>(c1);
}

template <bool NAP> __device__ __forceinline__ void lds_wait1(volatile LAS unsigned* p) {
#pragma unroll 1
  for (unsigned n = 0; n < SPIN_L; ++n) { if (*p == 1u) break; if (NAP) __builtin_amdgcn_s_sleep(1); }
  CBAR();
}
__device__ __forceinline__ void lds_wait2(volatile LAS unsigned* p) {
#pragma unroll 1
  for (unsigned n = 0; n < SPIN_L; ++n) { const v2u f = *(volatile LAS v2u*)p; if ((f[0] & f[1]) == 1u) break; __builtin_amdgcn_s_sleep(1); }
  CBAR();
}
template <bool NAP> __device__ __forceinline__ void lds_wait4(volatile LAS unsigned* p) {
#pragma unroll 1
  for (unsigned n = 0; n < SPIN_L; ++n) {
    const v4u f = *(volatile LAS v4u*)p;
    if ((f[0] & f[1] & f[2] & f[3]) == 1u) break;
    if (NAP) __builtin_amdgcn_s_sleep(1);
  }
  CBAR();
}
__device__ __forceinline__ void wait_ticks(unsigned long long t0, unsigned ticks) {
#pragma unroll 1
  while (__builtin_amdgcn_s_memrealtime() - t0 < ticks) __builtin_amdgcn_s_sleep(1);
}
#define LOAD_EPOCH() __hip_atomic_load((gu32*)(ws + WS_EPOCH), RLX_AGENT)
#define TAGOK(v) ((v)[1] == tag && (v)[3] == tag)
#define LD16(off) __builtin_amdgcn_raw_buffer_load_b128(rs, (off), 0, 16)
#define ST16(a, c, off) __builtin_amdgcn_raw_buffer_store_b128(v4u{f2u(a), tag, f2u(c), tag}, rs, (off), 0, 16)
#define RELEASED() ({ const v2u f_ = *(volatile LAS v2u*)(flag + 24); __builtin_amdgcn_readfirstlane(f_[0] & f_[1]) == 1u; })
#define CHUNK(u) ((((u) + 2 * g) & 15) * 32 + ((s + 5 * (u) + 3 * g) & 31))

__global__ void __launch_bounds__(768, 3)
fused_decode(const u16* __restrict__ x, const u16* __restrict__ kc, const u16* __restrict__ vc, const u16* __restrict__ wq,
             const u16* __restrict__ wo, const u16* __restrict__ w1, const u16* __restrict__ b1, const u16* __restrict__ w2,
             const u16* __restrict__ b2, u16* __restrict__ out, unsigned char* ws) {
  __shared__ __attribute__((aligned(16))) unsigned lds_[L_END];
  LAS unsigned* lds = (LAS unsigned*)lds_;
  volatile LAS unsigned* flag = (volatile LAS unsigned*)lds;
  const int tid = threadIdx.x, lane = tid & 63, wave = __builtin_amdgcn_readfirstlane(tid >> 6);
  const int b = blockIdx.x, g = b & 7, s = b >> 3;
  unsigned long long t_begin;
  asm volatile("s_memrealtime %0" : "=s"(t_begin));
  unsigned pf_ = 0;
  {
    const unsigned long long pc = __builtin_amdgcn_s_getpc() & ~255ull;
    const unsigned off = tid * 64u;
    if (off < CODEPF) pf_ = *(const GAS unsigned*)(pc + off);
  }
  asm volatile("s_waitcnt lgkmcnt(0)" : "+s"(t_begin)
               : "s"(x), "s"(kc), "s"(vc), "s"(wq), "s"(wo), "s"(w1), "s"(b1), "s"(w2), "s"(b2), "s"(out), "s"(ws) : "memory");
  const auto rs = MKRS(ws, WS_END);

  if (wave < 4) {
    __builtin_amdgcn_s_setprio(3);
    if (wave < 2) {
      float xv[8], xr[7]; v4u w[7]; u16 xb[7];
      const int row0 = (s >> 4) * 224 + wave * 112 + (lane >> 2);
      const v4u xa = *reinterpret_cast<const v4u*>(x + (lane < 56 ? lane : 55) * 8);
#pragma unroll
      for (int i = 0; i < 7; ++i) xb[i] = x[row0 + 16 * i];
      const unsigned ep = LOAD_EPOCH();
      SB();
      const auto rw = MKRS(wq, D * 4096 * 2);
      const unsigned wo_ = (row0 * 4096 + g * 512 + (s & 15) * 32 + (lane & 3) * 8) * 2;
#pragma unroll
      for (int i = 0; i < 7; ++i) { w[i] = NTLD(rw, wo_, i * 16 * 4096 * 2); SB(); }
      __builtin_amdgcn_s_barrier();
      SB();
      const unsigned tag = ep + 1u;
#pragma unroll
      for (int k = 0; k < 4; ++k) { xv[2 * k] = lo2f(xa[k]); xv[2 * k + 1] = hi2f(xa[k]); }
      if (wave == 0) flag[26] = tag;
      float m, inv;
      ln_stats8(xv, lane < 56, m, inv);
#pragma unroll
      for (int i = 0; i < 7; ++i) xr[i] = ln_apply(lo2f(xb[i]), m, inv);
      SB();
      float c0, c1;
      gemv32(xr, w, c0, c1);
      const int cpair = 4 * (lane & 3) + (lane >> 4);
      if (wave == 1) {
        if ((lane & 12) == 0) *(LAS v2f*)(lds + L_QP + 2 * cpair) = v2f{c0, c1};
        CBAR();
        flag[2] = 1u;
      } else {
        lds_wait1<false>(flag + 2);
        const v2f o = *(const LAS v2f*)(lds + L_QP + 2 * cpair);
        if ((lane & 12) == 0) ST16(c0 + o[0], c1 + o[1], WS_QG + ((g * 2 + (s >> 4)) * 512 + (s & 15) * 32 + 2 * cpair) * 8);
      }
      wait_ticks(__builtin_amdgcn_s_memrealtime(), QMIN);
      wait_ticks(t_begin, QDEAD);
      {
        const unsigned qoff = WS_QG + (g * 1024 + wave * 256 + lane * 4) * 8;
        v4u a0 = LD16(qoff), a1 = LD16(qoff + 16), a2 = LD16(qoff + 4096), a3 = LD16(qoff + 4096 + 16);
        SB();
        flag[24 + wave] = 1u;
        SB();
#pragma unroll 1
        for (unsigned n = 0; n < SPIN_G; ++n) {
          const bool k0 = TAGOK(a0) && TAGOK(a1), k1 = TAGOK(a2) && TAGOK(a3);
          if (__all(k0 && k1)) break;
          CBAR();
          if (!k0) { a0 = LD16(qoff); a1 = LD16(qoff + 16); }
          if (!k1) { a2 = LD16(qoff + 4096); a3 = LD16(qoff + 4096 + 16); }
        }
        *(LAS v2u*)(lds + L_Q + wave * 128 + lane * 2) =
            v2u{pack_bf16(u2f(a0[0]) + u2f(a2[0]), u2f(a0[2]) + u2f(a2[2])), pack_bf16(u2f(a1[0]) + u2f(a3[0]), u2f(a1[2]) + u2f(a3[2]))};
        CBAR();
        flag[wave] = 1u;
      }
      if (wave == 0) {
        lds_wait4<true>(flag + 28);
        lds_wait4<false>(flag + 8);
        const unsigned long long t_pub = __builtin_amdgcn_s_memrealtime();
        const unsigned soff = WS_SG + (g * 32 + (lane >> 2)) * 128 + (s >> 4) * 16;
        const unsigned ooff = WS_OG + ((g * 16 + (s >> 1)) * 32 + (lane >> 2)) * 128 + (s & 1) * 64 + (lane & 3) * 16;
        v4u st0 = LD16(soff), st1 = LD16(soff + 2048);
#pragma unroll 1
        for (unsigned n = 0; n < SPIN_G; ++n) {
          const bool k0 = TAGOK(st0), k1 = TAGOK(st1);
          if (__all(k0 && k1)) break;
          CBAR();
          if (!k0) st0 = LD16(soff);
          if (!k1) st1 = LD16(soff + 2048);
        }
        float cA, cB;
        {
          float M = fmaxf(u2f(st0[0]), u2f(st1[0]));
          M = fmaxf(M, dpp<ROR4>(M)); M = fmaxf(M, dpp<ROR8>(M));
          M = max32(max16(M));
          cA = __builtin_amdgcn_exp2f((u2f(st0[0]) - M) * L2E); cB = __builtin_amdgcn_exp2f((u2f(st1[0]) - M) * L2E);
          float L = fmaf(cA, u2f(st0[2]), cB * u2f(st1[2]));
          L = tr32(L, L); L = tr16(L, L); L += dpp<ROR8>(L); L += dpp<ROR4>(L);
          const float il = 1.f / L;
          cA *= il; cB *= il;
          asm volatile("" : "+v"(cA), "+v"(cB));
        }
        SB();
        wait_ticks(t_pub, ODELAY);
        v4u oa = LD16(ooff), ob = LD16(ooff + 2048);
        SB();
        flag[21] = 1u;
        SB();
#pragma unroll 1
        for (unsigned n = 0; n < SPIN_G; ++n) {
          const bool k0 = TAGOK(oa), k1 = TAGOK(ob);
          if (__all(k0 && k1)) break;
          CBAR();
          if (!k0) oa = LD16(ooff);
          if (!k1) ob = LD16(ooff + 2048);
        }
        const float e0 = fmaf(cA, lo2f(oa[0]), cB * lo2f(ob[0])), e1 = fmaf(cA, hi2f(oa[0]), cB * hi2f(ob[0]));
        const float e2 = fmaf(cA, lo2f(oa[2]), cB * lo2f(ob[2])), e3 = fmaf(cA, hi2f(oa[2]), cB * hi2f(ob[2]));
        float t = tr16(tr32(e0, e2), tr32(e1, e3));
        t += dpp<ROR8>(t);
        t += dpp<ROR4>(t);
        if ((lane & 12) == 0) lds[L_O + 4 * (lane & 3) + (lane >> 4)] = f2u(rbf(t));
        CBAR();
        flag[20] = 1u;
      }
    } else if (wave == 2) {
      const int u = s >> 2, qd = s & 3;
      if (u < 7) {
        const int cb = g * 7 + u;
        float xv[8], xr[7]; v4u w[7]; u16 xb[7];
        const int row0 = qd * 112 + (lane >> 2);
        const v4u xa = *reinterpret_cast<const v4u*>(x + (lane < 56 ? lane : 55) * 8);
#pragma unroll
        for (int i = 0; i < 7; ++i) xb[i] = x[row0 + 16 * i];
        const unsigned ep = LOAD_EPOCH();
        const unsigned bb = *reinterpret_cast<const unsigned*>(b1 + cb * 32 + qd * 8 + 2 * (lane & 3));
        SB();
        __builtin_amdgcn_s_barrier();
        SB();
        const auto rw = MKRS(w1, D * F * 2);
        const unsigned wo_ = (row0 * F + cb * 32 + (lane & 3) * 8) * 2;
#pragma unroll
        for (int i = 0; i < 7; ++i) { w[i] = NTLD(rw, wo_, i * 16 * F * 2); SB(); }
        __builtin_amdgcn_s_setprio(1);
        const unsigned tag = ep + 1u;
#pragma unroll
        for (int k = 0; k < 4; ++k) { xv[2 * k] = lo2f(xa[k]); xv[2 * k + 1] = hi2f(xa[k]); }
        float m, inv, m2, inv2;
        ln_stats8(xv, lane < 56, m, inv);
#pragma unroll
        for (int i = 0; i < 8; ++i) xv[i] = ln_apply(xv[i], m, inv);
        ln_stats8(xv, lane < 56, m2, inv2);
#pragma unroll
        for (int i = 0; i < 7; ++i) xr[i] = ln_apply(ln_apply(lo2f(xb[i]), m, inv), m2, inv2);
        SB();
        float c0, c1;
        gemv32(xr, w, c0, c1);
        if ((lane & 12) == 0) ST16(c0, c1, WS_ZG + ((cb * 4 + qd) * 32 + 2 * (4 * (lane & 3) + (lane >> 4))) * 8);
        lds_wait2(flag);
        const unsigned zoff = WS_ZG + ((cb * 4 + ((lane >> 2) & 3)) * 32 + qd * 8 + 2 * (lane & 3)) * 8;
        v4u z;
#pragma unroll 1
        for (unsigned n = 0; n < SPIN_G; ++n) {
          CBAR();
          z = LD16(zoff);
          if (__all(TAGOK(z))) break;
          __builtin_amdgcn_s_sleep(8);
        }
        float z0 = u2f(z[0]), z1 = u2f(z[2]);
        z0 += dpp<ROR8>(z0); z1 += dpp<ROR8>(z1);
        z0 += dpp<ROR4>(z0); z1 += dpp<ROR4>(z1);
        z0 = rbf(rbf(z0) + lo2f(bb));
        z1 = rbf(rbf(z1) + hi2f(bb));
        const float u0 = 0.7978845608028654f * (z0 + 0.044715f * z0 * z0 * z0);
        const float u1 = 0.7978845608028654f * (z1 + 0.044715f * z1 * z1 * z1);
        const float t0 = 1.f - 2.f / (__builtin_amdgcn_exp2f(2.f * L2E * u0) + 1.f);
        const float t1 = 1.f - 2.f / (__builtin_amdgcn_exp2f(2.f * L2E * u1) + 1.f);
        const float h0 = rbf(0.5f * z0 * (1.f + t0)), h1 = rbf(0.5f * z1 * (1.f + t1));
        lds_wait4<true>(flag + 28);
        v4u r2[8];
        const auto rw2 = MKRS(w2, D * F * 2);
        const unsigned w2o = ((cb * 32 + qd * 8) * D + (lane < 56 ? lane : 55) * 8) * 2;
#pragma unroll
        for (int r = 0; r < 8; ++r) { r2[r] = NTLD(rw2, w2o, r * D * 2); SB(); }
        v2f acc[4];
#pragma unroll
        for (int k = 0; k < 4; ++k) acc[k] = v2f{0.f, 0.f};
#pragma unroll
        for (int r = 0; r < 8; ++r) {
          const float hr = u2f(__builtin_amdgcn_readlane(f2u((r & 1) ? h1 : h0), r >> 1));
#pragma unroll
          for (int k = 0; k < 4; ++k) acc[k] = pkfma(v2f{lo2f(r2[r][k]), hi2f(r2[r][k])}, v2f{hr, hr}, acc[k]);
          SB();
        }
        if (lane < 56) {
          LAS v4f* mp = (LAS v4f*)(lds + L_MLP + lane * 8);
          mp[0] = v4f{acc[0][0], acc[0][1], acc[1][0], acc[1][1]};
          mp[1] = v4f{acc[2][0], acc[2][1], acc[3][0], acc[3][1]};
        }
      } else {
        __builtin_amdgcn_s_barrier();
        if (lane < 56) {
          LAS v4f* mp = (LAS v4f*)(lds + L_MLP + lane * 8);
          mp[0] = v4f{0.f, 0.f, 0.f, 0.f};
          mp[1] = v4f{0.f, 0.f, 0.f, 0.f};
        }
      }
      CBAR();
      flag[3] = 1u;
    } else {
      const int ell = (s >> 3) * 8 + g;
      if (lane < 32) flag[lane] = 0u;
      asm volatile("s_waitcnt lgkmcnt(0)" ::: "memory");
      __builtin_amdgcn_s_barrier();
      if (ell < 28) {
        const int cp = ell * 8 + (s & 7);
        const float base = lo2f(x[2 * cp + (lane >> 5)]) + lo2f(b2[2 * cp + (lane >> 5)]);
        lds_wait1<true>(flag + 21);
        lds_wait4<false>(flag + 12);
        const unsigned tag = flag[26];
        wait_ticks(__builtin_amdgcn_s_memrealtime(), FDELAY);
        const unsigned foff = WS_FG + lane * 3584 + cp * 16;
        v4u f0 = LD16(foff), f1 = LD16(foff + 64 * 3584), f2 = LD16(foff + 128 * 3584), f3 = LD16(foff + 192 * 3584);
#pragma unroll 1
        for (unsigned n = 0; n < SPIN_G; ++n) {
          const bool k0 = TAGOK(f0), k1 = TAGOK(f1), k2 = TAGOK(f2), k3 = TAGOK(f3);
          if (__all(k0 && k1 && k2 && k3)) break;
          CBAR();
          if (!k0) f0 = LD16(foff);
          if (!k1) f1 = LD16(foff + 64 * 3584);
          if (!k2) f2 = LD16(foff + 128 * 3584);
          if (!k3) f3 = LD16(foff + 192 * 3584);
        }
        const float s0 = (u2f(f0[0]) + u2f(f1[0])) + (u2f(f2[0]) + u2f(f3[0]));
        const float s1 = (u2f(f0[2]) + u2f(f1[2])) + (u2f(f2[2]) + u2f(f3[2]));
        float t = tr32(s0, s1);
        t = tr16(t, t);
        t = rowsum(t);
        if ((lane & 31) == 0) {
          const __bf16 r = (__bf16)(base + t);
          out[2 * cp + (lane >> 5)] = __builtin_bit_cast(u16, r);
          if (b == 0 && lane == 0) __hip_atomic_store((gu32*)(ws + WS_EPOCH), tag, RLX_AGENT);
        }
      }
    }
  } else if (wave < 8) {
    const int kw = wave - 4;
    __builtin_amdgcn_s_setprio(2);
    const auto rk = MKRS(kc + ((size_t)g * SK + (SK - WIN)) * DH, WIN * DH * 2);
    const unsigned ko = kw * 1024 + lane * 16;
    v4u kr[16];
    __builtin_amdgcn_s_barrier();
    SB();
#pragma unroll
    for (int i = 0; i < 16; ++i) {
      if (i >= WIN_K) { if (!RELEASED()) VMCNT(WIN_K - 1); }
      kr[i] = NTLD(rk, ko, CHUNK(i) * 4096); SB();
    }
    flag[16 + kw] = 1u;
    SB();
    lds_wait2(flag);
    const unsigned tag = flag[26];
    const v4u q0 = *(const LAS v4u*)(lds + L_Q + (lane & 31) * 4);
    const v4u q1 = *(const LAS v4u*)(lds + L_Q + 128 + (lane & 31) * 4);
    float y[16];
#pragma unroll
    for (int i = 0; i < 16; ++i) {
      float p0 = 0.f, p1 = 0.f;
#pragma unroll
      for (int k = 0; k < 4; ++k) { p0 = dot2(kr[i][k], q0[k], p0); p1 = dot2(kr[i][k], q1[k], p1); }
      y[i] = tr16(p0, p1);
      if ((i & 3) == 3) SB();
    }
    const bool b3 = lane & 8, b2_ = lane & 4, b1_ = lane & 2, b0_ = lane & 1;
#pragma unroll
    for (int i = 0; i < 8; ++i) y[i] = trrow<ROR8>(y[i], y[i + 8], b3);
#pragma unroll
    for (int i = 0; i < 4; ++i) y[i] = trrow<XOR7>(y[i], y[i + 4], b2_);
#pragma unroll
    for (int i = 0; i < 2; ++i) y[i] = trrow<XOR2>(y[i], y[i + 2], b1_);
    const float sc = rbf(trrow<XOR1>(y[0], y[1], b0_)) * 0.0625f;
    const float m = max32(rowmax(sc));
    const float p = __builtin_amdgcn_exp2f((sc - m) * L2E);
    {
      const int h = (lane >> 4) & 1;
      lds[L_P + ((2 * kw + (lane >> 5)) * 2 + h) * 20 + (lane & 15)] = f2u(p);
      if ((lane & 47) == 0) lds[L_WST + kw * 2 + h] = f2u(m);
      float l = rowsum(p);
      l = tr32(l, l);
      if ((lane & 47) == 0) lds[L_WST + 8 + kw * 2 + h] = f2u(l);
    }
    CBAR();
    flag[4 + kw] = 1u;
    if (kw == 0) {
      lds_wait4<true>(flag + 4);
      const int h = lane & 1;
      float mm[4], ll[4];
#pragma unroll
      for (int k = 0; k < 4; ++k) { mm[k] = u2f(lds[L_WST + k * 2 + h]); ll[k] = u2f(lds[L_WST + 8 + k * 2 + h]); }
      const float M = fmaxf(fmaxf(mm[0], mm[1]), fmaxf(mm[2], mm[3]));
      float L = 0.f;
#pragma unroll
      for (int k = 0; k < 4; ++k) L = fmaf(__builtin_amdgcn_exp2f((mm[k] - M) * L2E), ll[k], L);
      if (lane < 2) ST16(M, L, WS_SG + (g * 32 + s) * 128 + h * 16);
    }
    const int r4 = lane >> 4, c16 = lane & 15;
    const bool cact = kw < 3 || c16 < 8;
    const int cbase = kw * 128 + (cact ? c16 : (c16 & 7)) * 8;
    const int col = cbase + 2 * r4;
    const auto rwo = MKRS(wo, 4096 * D * 2);
    const unsigned woo = ((g * 512 + s * 16 + 4 * r4) * D + cbase) * 2;
    v4u wr[4];
    lds_wait4<true>(flag + 8);
#pragma unroll
    for (int i = 0; i < 4; ++i) { wr[i] = NTLD(rwo, woo, i * D * 2); SB(); }
    v2f wf[4][4];
#pragma unroll
    for (int i = 0; i < 4; ++i)
#pragma unroll
      for (int k = 0; k < 4; ++k) { wf[i][k] = v2f{lo2f(wr[i][k]), hi2f(wr[i][k])}; asm volatile("" : "+v"(wf[i][k])); }
    __builtin_amdgcn_s_setprio(3);
    lds_wait1<true>(flag + 3);
    const v2f mlp = *(const LAS v2f*)(lds + L_MLP + col);
    lds_wait1<false>(flag + 20);
    const v4f o = *(const LAS v4f*)(lds + L_O + 4 * r4);
    v2f acc[4];
#pragma unroll
    for (int k = 0; k < 4; ++k) acc[k] = wf[0][k] * v2f{o[0], o[0]};
#pragma unroll
    for (int i = 1; i < 4; ++i)
#pragma unroll
      for (int k = 0; k < 4; ++k) acc[k] = pkfma(wf[i][k], v2f{o[i], o[i]}, acc[k]);
    const float a0 = tr32(acc[0][0], acc[2][0]), a1 = tr32(acc[0][1], acc[2][1]);
    const float a2 = tr32(acc[1][0], acc[3][0]), a3 = tr32(acc[1][1], acc[3][1]);
    const float r0 = tr16(a0, a2) + mlp[0], r1 = tr16(a1, a3) + mlp[1];
    if (cact) ST16(r0, r1, WS_FG + b * 3584 + col * 8);
    CBAR();
    flag[12 + kw] = 1u;
  } else {
    const int vw = wave - 8;
    __builtin_amdgcn_s_setprio(1);
    const int ks = lane >> 3, c = lane & 7;
    const auto rv = MKRS(vc + ((size_t)g * SK + (SK - WIN)) * DH, WIN * DH * 2);
    const unsigned vo = ks * 512 + vw * 128 + c * 16;
    const unsigned ooff = WS_OG + ((g * 16 + ((lane >> 5) * 8 + 2 * vw + (c >> 2))) * 32 + s) * 128 +
                          ((c & 3) * 4 + 2 * ((lane >> 4) & 1) + ((lane >> 3) & 1)) * 8;
    v4u vr[16];
    __builtin_amdgcn_s_barrier();
    SB();
    lds_wait1<true>(flag + 16 + vw);
#pragma unroll
    for (int i = 0; i < 16; ++i) {
      if (i >= WIN_V && i < 10) { if (!RELEASED()) VMCNT(WIN_V - 1); }
      vr[i] = NTLD(rv, vo, CHUNK(i) * 4096); SB();
    }
    flag[28 + vw] = 1u;
    SB();
    __builtin_amdgcn_s_setprio(3);
    v4u fr[2][8];
#define VTRANS(bt, jj) _Pragma("unroll") for (int k = 0; k < 4; ++k) { \
      fr[bt][2 * k][jj] = __builtin_amdgcn_perm(vr[8 * bt + 2 * jj + 1][k], vr[8 * bt + 2 * jj][k], 0x05040100u); \
      fr[bt][2 * k + 1][jj] = __builtin_amdgcn_perm(vr[8 * bt + 2 * jj + 1][k], vr[8 * bt + 2 * jj][k], 0x07060302u); }
    VTRANS(0, 0) SB(); VTRANS(0, 1) SB(); VTRANS(0, 2) SB(); VTRANS(0, 3) SB();
    lds_wait4<true>(flag + 4);
    const unsigned tag = flag[26];
    bf16x8 pa[2];
    {
      const int m = lane & 15, r = lane >> 4, h = m >> 3, set = m & 1;
      const v4f wm0 = *(const LAS v4f*)(lds + L_WST), wm1 = *(const LAS v4f*)(lds + L_WST + 4);
      const float mk = u2f(lds[L_WST + r * 2 + h]);
      const LAS v4f* pp = (const LAS v4f*)(lds + L_P + ((2 * r + set) * 2 + h) * 20);
      const v4f p0 = pp[0], p1 = pp[1], p2 = pp[2], p3 = pp[3];
      const float M = h ? fmaxf(fmaxf(wm0[1], wm0[3]), fmaxf(wm1[1], wm1[3])) : fmaxf(fmaxf(wm0[0], wm0[2]), fmaxf(wm1[0], wm1[2]));
      const float f = (m & 2) ? 0.f : __builtin_amdgcn_exp2f((mk - M) * L2E);
      const v4u a0 = {pack_bf16(p0[0] * f, p0[1] * f), pack_bf16(p0[2] * f, p0[3] * f), pack_bf16(p1[0] * f, p1[1] * f), pack_bf16(p1[2] * f, p1[3] * f)};
      const v4u a1 = {pack_bf16(p2[0] * f, p2[1] * f), pack_bf16(p2[2] * f, p2[3] * f), pack_bf16(p3[0] * f, p3[1] * f), pack_bf16(p3[2] * f, p3[3] * f)};
      pa[0] = __builtin_bit_cast(bf16x8, a0); pa[1] = __builtin_bit_cast(bf16x8, a1);
    }
    v4f acc[8];
#pragma unroll
    for (int d = 0; d < 8; ++d)
      acc[d] = __builtin_amdgcn_mfma_f32_16x16x32_bf16(pa[0], __builtin_bit_cast(bf16x8, fr[0][d]), v4f{0.f, 0.f, 0.f, 0.f}, 0, 0, 0);
    SB();
    VTRANS(1, 0) SB(); VTRANS(1, 1) SB(); VTRANS(1, 2) SB(); VTRANS(1, 3)
#pragma unroll
    for (int d = 0; d < 8; ++d)
      acc[d] = __builtin_amdgcn_mfma_f32_16x16x32_bf16(pa[1], __builtin_bit_cast(bf16x8, fr[1][d]), acc[d], 0, 0, 0);
    const bool b4 = lane & 16, b3 = lane & 8;
    float t4[4];
#pragma unroll
    for (int k = 0; k < 4; ++k) {
      const float e0 = b4 ? acc[k + 4][0] : acc[k][0], e1 = b4 ? acc[k + 4][1] : acc[k][1];
      t4[k] = b3 ? e1 : e0;
    }
    const float r0 = trrow<ROR8>(t4[0], t4[2], b3), r1 = trrow<ROR8>(t4[1], t4[3], b3);
    __builtin_amdgcn_raw_buffer_store_b64(v2u{pack_bf16(r0, r1), tag}, rs, ooff, 0, 16);
    CBAR();
    flag[8 + vw] = 1u;
  }
  asm volatile("" ::"v"(pf_));
}

extern "C" void kernel_launch(void* const* d_in, const int* in_sizes, int n_in, void* d_out, int out_size, void* d_ws, size_t ws_size,
                              hipStream_t stream) {
  fused_decode<<<256, 768, 0, stream>>>((const u16*)d_in[0], (const u16*)d_in[1], (const u16*)d_in[2], (const u16*)d_in[3],
                                        (const u16*)d_in[4], (const u16*)d_in[6], (const u16*)d_in[7], (const u16*)d_in[8],
                                        (const u16*)d_in[9], (u16*)d_out, (unsigned char*)d_ws);
}
```

```cpp
#include <hip/hip_runtime.h>
#include <hip/hip_bf16.h>
#include <stdint.h>

#define GAS __attribute__((address_space(1)))
#define LAS __attribute__((address_space(3)))
typedef unsigned short u16;
typedef unsigned v4u __attribute__((ext_vector_type(4)));
typedef unsigned v2u __attribute__((ext_vector_type(2)));
typedef float v4f __attribute__((ext_vector_type(4)));
typedef float v2f __attribute__((ext_vector_type(2)));
typedef __bf16 bf16x2 __attribute__((ext_vector_type(2)));
typedef __bf16 bf16x8 __attribute__((ext_vector_type(8)));
typedef GAS unsigned gu32;
#define RLX_AGENT __ATOMIC_RELAXED, __HIP_MEMORY_SCOPE_AGENT
#define SB() __builtin_amdgcn_sched_barrier(0)
#define CBAR() asm volatile("" ::: "memory")
#define VMCNT(n) asm volatile("s_waitcnt vmcnt(%0)" ::"i"(n) : "memory")

#define WIN_K 4
#define WIN_V 2
#define QDEAD 210
#define QMIN 70
#define ODELAY 60
#define FDELAY 50
#define CODEPF 16128

constexpr int D = 448, SK = 32768, DH = 256, WIN = 4096, F = 1792;
constexpr float L2E = 1.4426950408889634f;
constexpr unsigned SPIN_G = 1u << 17;
constexpr unsigned SPIN_L = 1u << 21;

constexpr unsigned WS_EPOCH = 0;
constexpr unsigned WS_QG = 4096;
constexpr unsigned WS_SG = WS_QG + 65536;
constexpr unsigned WS_ZG = WS_SG + 32768;
constexpr unsigned WS_OG = 262144;
constexpr unsigned WS_FG = WS_OG + (512u << 10);
constexpr unsigned WS_END = WS_FG + 256u * 448u * 8u;

constexpr int L_Q = 32;
constexpr int L_P = L_Q + 256;
constexpr int L_WST = L_P + 16 * 20;
constexpr int L_QP = L_WST + 16;
constexpr int L_O = L_QP + 32;
constexpr int L_MLP = L_O + 16;
constexpr int L_END = L_MLP + 448;

__device__ __forceinline__ float u2f(unsigned v) { return __builtin_bit_cast(float, v); }
__device__ __forceinline__ unsigned f2u(float v) { return __builtin_bit_cast(unsigned, v); }
__device__ __forceinline__ float lo2f(unsigned v) { return u2f(v << 16); }
__device__ __forceinline__ float hi2f(unsigned v) { return u2f(v & 0xffff0000u); }
__device__ __forceinline__ unsigned pack_bf16(float lo, float hi) {
  bf16x2 r; r[0] = (__bf16)lo; r[1] = (__bf16)hi; return __builtin_bit_cast(unsigned, r);
}
__device__ __forceinline__ float rbf(float f) { return (float)(__bf16)f; }
__device__ __forceinline__ float dot2(unsigned a, unsigned b, float c) {
  return __builtin_amdgcn_fdot2_f32_bf16(__builtin_bit_cast(bf16x2, a), __builtin_bit_cast(bf16x2, b), c, false);
}
__device__ __forceinline__ v2f pkfma(v2f a, v2f b, v2f c) { return __builtin_elementwise_fma(a, b, c); }
template <int CTRL> __device__ __forceinline__ float dpp(float x) {
  return u2f((unsigned)__builtin_amdgcn_mov_dpp((int)f2u(x), CTRL, 0xf, 0xf, true));
}
constexpr int ROR8 = 0x128, ROR4 = 0x124, ROR2 = 0x122, ROR1 = 0x121, XOR7 = 0x141, XOR2 = 0x4E, XOR1 = 0xB1;
__device__ __forceinline__ float tr32(float a, float b) {
  auto r = __builtin_amdgcn_permlane32_swap(f2u(a), f2u(b), false, false); return u2f(r[0]) + u2f(r[1]);
}
__device__ __forceinline__ float tr16(float a, float b) {
  auto r = __builtin_amdgcn_permlane16_swap(f2u(a), f2u(b), false, false); return u2f(r[0]) + u2f(r[1]);
}
__device__ __forceinline__ float rowsum(float v) { v += dpp<ROR8>(v); v += dpp<ROR4>(v); v += dpp<ROR2>(v); v += dpp<ROR1>(v); return v; }
__device__ __forceinline__ float rowmax(float v) {
  v = fmaxf(v, dpp<ROR8>(v)); v = fmaxf(v, dpp<ROR4>(v)); v = fmaxf(v, dpp<ROR2>(v)); v = fmaxf(v, dpp<ROR1>(v)); return v;
}
__device__ __forceinline__ float max16(float v) {
  auto r = __builtin_amdgcn_permlane16_swap(f2u(v), f2u(v), false, false); return fmaxf(u2f(r[0]), u2f(r[1]));
}
__device__ __forceinline__ float max32(float v) {
  auto r = __builtin_amdgcn_permlane32_swap(f2u(v), f2u(v), false, false); return fmaxf(u2f(r[0]), u2f(r[1]));
}
template <int CTRL> __device__ __forceinline__ float trrow(float a, float b, bool bit) {
  const float keep = bit ? b : a, send = bit ? a : b;
  return keep + dpp<CTRL>(send);
}
#define MKRS(p, bytes) __builtin_amdgcn_make_buffer_rsrc((void*)(p), 0, (bytes), 0x00020000)
#define NTLD(rsrc, voff, soff) __builtin_amdgcn_raw_buffer_load_b128((rsrc), (voff), (soff), 2)

__device__ __forceinline__ void ln_stats8(const float (&v)[8], bool act, float& m, float& inv) {
  const float c = u2f(__builtin_amdgcn_readfirstlane(f2u(v[0])));
  float d[8];
#pragma unroll
  for (int i = 0; i < 8; ++i) d[i] = v[i] - c;
  float s = ((d[0] + d[1]) + (d[2] + d[3])) + ((d[4] + d[5]) + (d[6] + d[7]));
  float q = fmaf(d[0], d[0], fmaf(d[1], d[1], fmaf(d[2], d[2], d[3] * d[3]))) + fmaf(d[4], d[4], fmaf(d[5], d[5], fmaf(d[6], d[6], d[7] * d[7])));
  s = act ? s : 0.f; q = act ? q : 0.f;
  float t = tr32(s, q);
  t = tr16(t, t);
  t = rowsum(t);
  const float S = u2f(__builtin_amdgcn_readlane(f2u(t), 0)) * (1.f / D), Q = u2f(__builtin_amdgcn_readlane(f2u(t), 32)) * (1.f / D);
  const float var = rbf(fmaxf(fmaf(-S, S, Q), 0.f));
  inv = 1.f / rbf(sqrtf(rbf(var + 1e-5f)));
  m = rbf(c + S);
}
__device__ __forceinline__ float ln_apply(float v, float m, float inv) { return rbf(rbf(v - m) * inv); }

__device__ __forceinline__ void gemv32(const float (&xv)[7], const v4u (&w)[7], float& c0, float& c1) {
  v2f acc[4];
#pragma unroll
  for (int k = 0; k < 4; ++k) acc[k] = v2f{0.f, 0.f};
#pragma unroll
  for (int i = 0; i < 7; ++i) {
#pragma unroll
    for (int k = 0; k < 4; ++k) acc[k] = pkfma(v2f{lo2f(w[i][k]), hi2f(w[i][k])}, v2f{xv[i], xv[i]}, acc[k]);
    SB();
  }
  const float a0 = tr32(acc[0][0], acc[2][0]), a1 = tr32(acc[0][1], acc[2][1]);
  const float a2 = tr32(acc[1][0], acc[3][0]), a3 = tr32(acc[1][1], acc[3][1]);
  c0 = tr16(a0, a2); c1 = tr16(a1, a3);
  c0 += dpp<ROR8>(c0); c1 += dpp<ROR8>(c1);
  c0 += dpp<ROR4>(c0); c1 += dpp<ROR4>(c1);
}

template <bool NAP> __device__ __forceinline__ void lds_wait1(volatile LAS unsigned* p) {
#pragma unroll 1
  for (unsigned n = 0; n < SPIN_L; ++n) { if (*p == 1u) break; if (NAP) __builtin_amdgcn_s_sleep(1); }
  CBAR();
}
__device__ __forceinline__ void lds_wait2(volatile LAS unsigned* p) {
#pragma unroll 1
  for (unsigned n = 0; n < SPIN_L; ++n) { const v2u f = *(volatile LAS v2u*)p; if ((f[0] & f[1]) == 1u) break; __builtin_amdgcn_s_sleep(1); }
  CBAR();
}
template <bool NAP> __device__ __forceinline__ void lds_wait4(volatile LAS unsigned* p) {
#pragma unroll 1
  for (unsigned n = 0; n < SPIN_L; ++n) {
    const v4u f = *(volatile LAS v4u*)p;
    if ((f[0] & f[1] & f[2] & f[3]) == 1u) break;
    if (NAP) __builtin_amdgcn_s_sleep(1);
  }
  CBAR();
}
__device__ __forceinline__ void wait_ticks(unsigned long long t0, unsigned ticks) {
#pragma unroll 1
  while (__builtin_amdgcn_s_memrealtime() - t0 < ticks) __builtin_amdgcn_s_sleep(1);
}
#define LOAD_EPOCH() __hip_atomic_load((gu32*)(ws + WS_EPOCH), RLX_AGENT)
#define TAGOK(v) ((v)[1] == tag && (v)[3] == tag)
#define LD16(off) __builtin_amdgcn_raw_buffer_load_b128(rs, (off), 0, 16)
#define ST16(a, c, off) __builtin_amdgcn_raw_buffer_store_b128(v4u{f2u(a), tag, f2u(c), tag}, rs, (off), 0, 16)
#define RELEASED() ({ const v2u f_ = *(volatile LAS v2u*)(flag + 24); __builtin_amdgcn_readfirstlane(f_[0] & f_[1]) == 1u; })
#define CHUNK(u) ((((u) + 2 * g) & 15) * 32 + ((s + 5 * (u) + 3 * g) & 31))

__global__ void __launch_bounds__(768, 3)
fused_decode(const u16* __restrict__ x, const u16* __restrict__ kc, const u16* __restrict__ vc, const u16* __restrict__ wq,
             const u16* __restrict__ wo, const u16* __restrict__ w1, const u16* __restrict__ b1, const u16* __restrict__ w2,
             const u16* __restrict__ b2, u16* __restrict__ out, unsigned char* ws) {
  __shared__ __attribute__((aligned(16))) unsigned lds_[L_END];
  LAS unsigned* lds = (LAS unsigned*)lds_;
  volatile LAS unsigned* flag = (volatile LAS unsigned*)lds;
  const int tid = threadIdx.x, lane = tid & 63, wave = __builtin_amdgcn_readfirstlane(tid >> 6);
  const int b = blockIdx.x, g = b & 7, s = b >> 3;
  unsigned pf_ = 0;
  {
    const unsigned long long pc = __builtin_amdgcn_s_getpc() & ~255ull;
    const unsigned off = tid * 64u;
    if (off < CODEPF) pf_ = *(const GAS unsigned*)(pc + off);
  }
  asm volatile("s_waitcnt lgkmcnt(0)" :
               : "s"(x), "s"(kc), "s"(vc), "s"(wq), "s"(wo), "s"(w1), "s"(b1), "s"(w2), "s"(b2), "s"(out), "s"(ws) : "memory");
  const auto rs = MKRS(ws, WS_END);

  if (wave < 4) {
    __builtin_amdgcn_s_setprio(3);
    if (wave < 2) {
      float xv[8], xr[7]; v4u w[7]; u16 xb[7];
      const int row0 = (s >> 4) * 224 + wave * 112 + (lane >> 2);
      const v4u xa = *reinterpret_cast<const v4u*>(x + (lane < 56 ? lane : 55) * 8);
#pragma unroll
      for (int i = 0; i < 7; ++i) xb[i] = x[row0 + 16 * i];
      const unsigned ep = LOAD_EPOCH();
      SB();
      const auto rw = MKRS(wq, D * 4096 * 2);
      const unsigned wo_ = (row0 * 4096 + g * 512 + (s & 15) * 32 + (lane & 3) * 8) * 2;
#pragma unroll
      for (int i = 0; i < 7; ++i) { w[i] = NTLD(rw, wo_, i * 16 * 4096 * 2); SB(); }
      __builtin_amdgcn_s_barrier();
      SB();
      const unsigned tag = ep + 1u;
#pragma unroll
      for (int k = 0; k < 4; ++k) { xv[2 * k] = lo2f(xa[k]); xv[2 * k + 1] = hi2f(xa[k]); }
      asm volatile("" : "+v"(xv[0]));
      SB();
      const unsigned long long t_x = __builtin_amdgcn_s_memrealtime();
      SB();
      if (wave == 0) flag[26] = tag;
      float m, inv;
      ln_stats8(xv, lane < 56, m, inv);
#pragma unroll
      for (int i = 0; i < 7; ++i) xr[i] = ln_apply(lo2f(xb[i]), m, inv);
      SB();
      float c0, c1;
      gemv32(xr, w, c0, c1);
      const int cpair = 4 * (lane & 3) + (lane >> 4);
      if (wave == 1) {
        if ((lane & 12) == 0) *(LAS v2f*)(lds + L_QP + 2 * cpair) = v2f{c0, c1};
        CBAR();
        flag[2] = 1u;
      } else {
        lds_wait1<false>(flag + 2);
        const v2f o = *(const LAS v2f*)(lds + L_QP + 2 * cpair);
        if ((lane & 12) == 0) ST16(c0 + o[0], c1 + o[1], WS_QG + ((g * 2 + (s >> 4)) * 512 + (s & 15) * 32 + 2 * cpair) * 8);
      }
      wait_ticks(__builtin_amdgcn_s_memrealtime(), QMIN);
      wait_ticks(t_x, QDEAD);
      {
        const unsigned qoff = WS_QG + (g * 1024 + wave * 256 + lane * 4) * 8;
        v4u a0 = LD16(qoff), a1 = LD16(qoff + 16), a2 = LD16(qoff + 4096), a3 = LD16(qoff + 4096 + 16);
        SB();
        flag[24 + wave] = 1u;
        SB();
#pragma unroll 1
        for (unsigned n = 0; n < SPIN_G; ++n) {
          const bool k0 = TAGOK(a0) && TAGOK(a1), k1 = TAGOK(a2) && TAGOK(a3);
          if (__all(k0 && k1)) break;
          CBAR();
          if (!k0) { a0 = LD16(qoff); a1 = LD16(qoff + 16); }
          if (!k1) { a2 = LD16(qoff + 4096); a3 = LD16(qoff + 4096 + 16); }
        }
        *(LAS v2u*)(lds + L_Q + wave * 128 + lane * 2) =
            v2u{pack_bf16(u2f(a0[0]) + u2f(a2[0]), u2f(a0[2]) + u2f(a2[2])), pack_bf16(u2f(a1[0]) + u2f(a3[0]), u2f(a1[2]) + u2f(a3[2]))};
        CBAR();
        flag[wave] = 1u;
      }
      if (wave == 0) {
        lds_wait4<true>(flag + 28);
        lds_wait4<false>(flag + 8);
        const unsigned long long t_pub = __builtin_amdgcn_s_memrealtime();
        const unsigned soff = WS_SG + (g * 32 + (lane >> 2)) * 128 + (s >> 4) * 16;
        const unsigned ooff = WS_OG + ((g * 16 + (s >> 1)) * 32 + (lane >> 2)) * 128 + (s & 1) * 64 + (lane & 3) * 16;
        v4u st0 = LD16(soff), st1 = LD16(soff + 2048);
#pragma unroll 1
        for (unsigned n = 0; n < SPIN_G; ++n) {
          const bool k0 = TAGOK(st0), k1 = TAGOK(st1);
          if (__all(k0 && k1)) break;
          CBAR();
          if (!k0) st0 = LD16(soff);
          if (!k1) st1 = LD16(soff + 2048);
        }
        float cA, cB;
        {
          float M = fmaxf(u2f(st0[0]), u2f(st1[0]));
          M = fmaxf(M, dpp<ROR4>(M)); M = fmaxf(M, dpp<ROR8>(M));
          M = max32(max16(M));
          cA = __builtin_amdgcn_exp2f((u2f(st0[0]) - M) * L2E); cB = __builtin_amdgcn_exp2f((u2f(st1[0]) - M) * L2E);
          float L = fmaf(cA, u2f(st0[2]), cB * u2f(st1[2]));
          L = tr32(L, L); L = tr16(L, L); L += dpp<ROR8>(L); L += dpp<ROR4>(L);
          const float il = 1.f / L;
          cA *= il; cB *= il;
          asm volatile("" : "+v"(cA), "+v"(cB));
        }
        SB();
        wait_ticks(t_pub, ODELAY);
        v4u oa = LD16(ooff), ob = LD16(ooff + 2048);
        SB();
        flag[21] = 1u;
        SB();
#pragma unroll 1
        for (unsigned n = 0; n < SPIN_G; ++n) {
          const bool k0 = TAGOK(oa), k1 = TAGOK(ob);
          if (__all(k0 && k1)) break;
          CBAR();
          if (!k0) oa = LD16(ooff);
          if (!k1) ob = LD16(ooff + 2048);
        }
        const float e0 = fmaf(cA, lo2f(oa[0]), cB * lo2f(ob[0])), e1 = fmaf(cA, hi2f(oa[0]), cB * hi2f(ob[0]));
        const float e2 = fmaf(cA, lo2f(oa[2]), cB * lo2f(ob[2])), e3 = fmaf(cA, hi2f(oa[2]), cB * hi2f(ob[2]));
        float t = tr16(tr32(e0, e2), tr32(e1, e3));
        t += dpp<ROR8>(t);
        t += dpp<ROR4>(t);
        if ((lane & 12) == 0) lds[L_O + 4 * (lane & 3) + (lane >> 4)] = f2u(rbf(t));
        CBAR();
        flag[20] = 1u;
      }
    } else if (wave == 2) {
      const int u = s >> 2, qd = s & 3;
      if (u < 7) {
        const int cb = g * 7 + u;
        float xv[8], xr[7]; v4u w[7]; u16 xb[7];
        const int row0 = qd * 112 + (lane >> 2);
        const v4u xa = *reinterpret_cast<const v4u*>(x + (lane < 56 ? lane : 55) * 8);
#pragma unroll
        for (int i = 0; i < 7; ++i) xb[i] = x[row0 + 16 * i];
        const unsigned ep = LOAD_EPOCH();
        const unsigned bb = *reinterpret_cast<const unsigned*>(b1 + cb * 32 + qd * 8 + 2 * (lane & 3));
        SB();
        __builtin_amdgcn_s_barrier();
        SB();
        const auto rw = MKRS(w1, D * F * 2);
        const unsigned wo_ = (row0 * F + cb * 32 + (lane & 3) * 8) * 2;
#pragma unroll
        for (int i = 0; i < 7; ++i) { w[i] = NTLD(rw, wo_, i * 16 * F * 2); SB(); }
        __builtin_amdgcn_s_setprio(1);
        const unsigned tag = ep + 1u;
#pragma unroll
        for (int k = 0; k < 4; ++k) { xv[2 * k] = lo2f(xa[k]); xv[2 * k + 1] = hi2f(xa[k]); }
        float m, inv, m2, inv2;
        ln_stats8(xv, lane < 56, m, inv);
#pragma unroll
        for (int i = 0; i < 8; ++i) xv[i] = ln_apply(xv[i], m, inv);
        ln_stats8(xv, lane < 56, m2, inv2);
#pragma unroll
        for (int i = 0; i < 7; ++i) xr[i] = ln_apply(ln_apply(lo2f(xb[i]), m, inv), m2, inv2);
        SB();
        float c0, c1;
        gemv32(xr, w, c0, c1);
        if ((lane & 12) == 0) ST16(c0, c1, WS_ZG + ((cb * 4 + qd) * 32 + 2 * (4 * (lane & 3) + (lane >> 4))) * 8);
        lds_wait2(flag);
        const unsigned zoff = WS_ZG + ((cb * 4 + ((lane >> 2) & 3)) * 32 + qd * 8 + 2 * (lane & 3)) * 8;
        v4u z;
#pragma unroll 1
        for (unsigned n = 0; n < SPIN_G; ++n) {
          CBAR();
          z = LD16(zoff);
          if (__all(TAGOK(z))) break;
          __builtin_amdgcn_s_sleep(8);
        }
        float z0 = u2f(z[0]), z1 = u2f(z[2]);
        z0 += dpp<ROR8>(z0); z1 += dpp<ROR8>(z1);
        z0 += dpp<ROR4>(z0); z1 += dpp<ROR4>(z1);
        z0 = rbf(rbf(z0) + lo2f(bb));
        z1 = rbf(rbf(z1) + hi2f(bb));
        const float u0 = 0.7978845608028654f * (z0 + 0.044715f * z0 * z0 * z0);
        const float u1 = 0.7978845608028654f * (z1 + 0.044715f * z1 * z1 * z1);
        const float t0 = 1.f - 2.f / (__builtin_amdgcn_exp2f(2.f * L2E * u0) + 1.f);
        const float t1 = 1.f - 2.f / (__builtin_amdgcn_exp2f(2.f * L2E * u1) + 1.f);
        const float h0 = rbf(0.5f * z0 * (1.f + t0)), h1 = rbf(0.5f * z1 * (1.f + t1));
        lds_wait4<true>(flag + 28);
        v4u r2[8];
        const auto rw2 = MKRS(w2, D * F * 2);
        const unsigned w2o = ((cb * 32 + qd * 8) * D + (lane < 56 ? lane : 55) * 8) * 2;
#pragma unroll
        for (int r = 0; r < 8; ++r) { r2[r] = NTLD(rw2, w2o, r * D * 2); SB(); }
        v2f acc[4];
#pragma unroll
        for (int k = 0; k < 4; ++k) acc[k] = v2f{0.f, 0.f};
#pragma unroll
        for (int r = 0; r < 8; ++r) {
          const float hr = u2f(__builtin_amdgcn_readlane(f2u((r & 1) ? h1 : h0), r >> 1));
#pragma unroll
          for (int k = 0; k < 4; ++k) acc[k] = pkfma(v2f{lo2f(r2[r][k]), hi2f(r2[r][k])}, v2f{hr, hr}, acc[k]);
          SB();
        }
        if (lane < 56) {
          LAS v4f* mp = (LAS v4f*)(lds + L_MLP + lane * 8);
          mp[0] = v4f{acc[0][0], acc[0][1], acc[1][0], acc[1][1]};
          mp[1] = v4f{acc[2][0], acc[2][1], acc[3][0], acc[3][1]};
        }
      } else {
        __builtin_amdgcn_s_barrier();
        if (lane < 56) {
          LAS v4f* mp = (LAS v4f*)(lds + L_MLP + lane * 8);
          mp[0] = v4f{0.f, 0.f, 0.f, 0.f};
          mp[1] = v4f{0.f, 0.f, 0.f, 0.f};
        }
      }
      CBAR();
      flag[3] = 1u;
    } else {
      const int ell = (s >> 3) * 8 + g;
      if (lane < 32) flag[lane] = 0u;
      asm volatile("s_waitcnt lgkmcnt(0)" ::: "memory");
      __builtin_amdgcn_s_barrier();
      if (ell < 28) {
        const int cp = ell * 8 + (s & 7);
        const float base = lo2f(x[2 * cp + (lane >> 5)]) + lo2f(b2[2 * cp + (lane >> 5)]);
        lds_wait1<true>(flag + 21);
        lds_wait4<false>(flag + 12);
        const unsigned tag = flag[26];
        wait_ticks(__builtin_amdgcn_s_memrealtime(), FDELAY);
        const unsigned foff = WS_FG + lane * 3584 + cp * 16;
        v4u f0 = LD16(foff), f1 = LD16(foff + 64 * 3584), f2 = LD16(foff + 128 * 3584), f3 = LD16(foff + 192 * 3584);
#pragma unroll 1
        for (unsigned n = 0; n < SPIN_G; ++n) {
          const bool k0 = TAGOK(f0), k1 = TAGOK(f1), k2 = TAGOK(f2), k3 = TAGOK(f3);
          if (__all(k0 && k1 && k2 && k3)) break;
          CBAR();
          if (!k0) f0 = LD16(foff);
          if (!k1) f1 = LD16(foff + 64 * 3584);
          if (!k2) f2 = LD16(foff + 128 * 3584);
          if (!k3) f3 = LD16(foff + 192 * 3584);
        }
        const float s0 = (u2f(f0[0]) + u2f(f1[0])) + (u2f(f2[0]) + u2f(f3[0]));
        const float s1 = (u2f(f0[2]) + u2f(f1[2])) + (u2f(f2[2]) + u2f(f3[2]));
        float t = tr32(s0, s1);
        t = tr16(t, t);
        t = rowsum(t);
        if ((lane & 31) == 0) {
          const __bf16 r = (__bf16)(base + t);
          out[2 * cp + (lane >> 5)] = __builtin_bit_cast(u16, r);
          if (b == 0 && lane == 0) __hip_atomic_store((gu32*)(ws + WS_EPOCH), tag, RLX_AGENT);
        }
      }
    }
  } else if (wave < 8) {
    const int kw = wave - 4;
    __builtin_amdgcn_s_setprio(2);
    const auto rk = MKRS(kc + ((size_t)g * SK + (SK - WIN)) * DH, WIN * DH * 2);
    const unsigned ko = kw * 1024 + lane * 16;
    v4u kr[16];
    __builtin_amdgcn_s_barrier();
    SB();
#pragma unroll
    for (int i = 0; i < 16; ++i) {
      if (i >= WIN_K) { if (!RELEASED()) VMCNT(WIN_K - 1); }
      kr[i] = NTLD(rk, ko, CHUNK(i) * 4096); SB();
    }
    flag[16 + kw] = 1u;
    SB();
    lds_wait2(flag);
    const unsigned tag = flag[26];
    const v4u q0 = *(const LAS v4u*)(lds + L_Q + (lane & 31) * 4);
    const v4u q1 = *(const LAS v4u*)(lds + L_Q + 128 + (lane & 31) * 4);
    float y[16];
#pragma unroll
    for (int i = 0; i < 16; ++i) {
      float p0 = 0.f, p1 = 0.f;
#pragma unroll
      for (int k = 0; k < 4; ++k) { p0 = dot2(kr[i][k], q0[k], p0); p1 = dot2(kr[i][k], q1[k], p1); }
      y[i] = tr16(p0, p1);
      if ((i & 3) == 3) SB();
    }
    const bool b3 = lane & 8, b2_ = lane & 4, b1_ = lane & 2, b0_ = lane & 1;
#pragma unroll
    for (int i = 0; i < 8; ++i) y[i] = trrow<ROR8>(y[i], y[i + 8], b3);
#pragma unroll
    for (int i = 0; i < 4; ++i) y[i] = trrow<XOR7>(y[i], y[i + 4], b2_);
#pragma unroll
    for (int i = 0; i < 2; ++i) y[i] = trrow<XOR2>(y[i], y[i + 2], b1_);
    const float sc = rbf(trrow<XOR1>(y[0], y[1], b0_)) * 0.0625f;
    const float m = max32(rowmax(sc));
    const float p = __builtin_amdgcn_exp2f((sc - m) * L2E);
    {
      const int h = (lane >> 4) & 1;
      lds[L_P + ((2 * kw + (lane >> 5)) * 2 + h) * 20 + (lane & 15)] = f2u(p);
      if ((lane & 47) == 0) lds[L_WST + kw * 2 + h] = f2u(m);
      float l = rowsum(p);
      l = tr32(l, l);
      if ((lane & 47) == 0) lds[L_WST + 8 + kw * 2 + h] = f2u(l);
    }
    CBAR();
    flag[4 + kw] = 1u;
    if (kw == 0) {
      lds_wait4<true>(flag + 4);
      const int h = lane & 1;
      float mm[4], ll[4];
#pragma unroll
      for (int k = 0; k < 4; ++k) { mm[k] = u2f(lds[L_WST + k * 2 + h]); ll[k] = u2f(lds[L_WST + 8 + k * 2 + h]); }
      const float M = fmaxf(fmaxf(mm[0], mm[1]), fmaxf(mm[2], mm[3]));
      float L = 0.f;
#pragma unroll
      for (int k = 0; k < 4; ++k) L = fmaf(__builtin_amdgcn_exp2f((mm[k] - M) * L2E), ll[k], L);
      if (lane < 2) ST16(M, L, WS_SG + (g * 32 + s) * 128 + h * 16);
    }
    const int r4 = lane >> 4, c16 = lane & 15;
    const bool cact = kw < 3 || c16 < 8;
    const int cbase = kw * 128 + (cact ? c16 : (c16 & 7)) * 8;
    const int col = cbase + 2 * r4;
    const auto rwo = MKRS(wo, 4096 * D * 2);
    const unsigned woo = ((g * 512 + s * 16 + 4 * r4) * D + cbase) * 2;
    v4u wr[4];
    lds_wait4<true>(flag + 8);
#pragma unroll
    for (int i = 0; i < 4; ++i) { wr[i] = NTLD(rwo, woo, i * D * 2); SB(); }
    v2f wf[4][4];
#pragma unroll
    for (int i = 0; i < 4; ++i)
#pragma unroll
      for (int k = 0; k < 4; ++k) { wf[i][k] = v2f{lo2f(wr[i][k]), hi2f(wr[i][k])}; asm volatile("" : "+v"(wf[i][k])); }
    __builtin_amdgcn_s_setprio(3);
    lds_wait1<true>(flag + 3);
    const v2f mlp = *(const LAS v2f*)(lds + L_MLP + col);
    lds_wait1<false>(flag + 20);
    const v4f o = *(const LAS v4f*)(lds + L_O + 4 * r4);
    v2f acc[4];
#pragma unroll
    for (int k = 0; k < 4; ++k) acc[k] = wf[0][k] * v2f{o[0], o[0]};
#pragma unroll
    for (int i = 1; i < 4; ++i)
#pragma unroll
      for (int k = 0; k < 4; ++k) acc[k] = pkfma(wf[i][k], v2f{o[i], o[i]}, acc[k]);
    const float a0 = tr32(acc[0][0], acc[2][0]), a1 = tr32(acc[0][1], acc[2][1]);
    const float a2 = tr32(acc[1][0], acc[3][0]), a3 = tr32(acc[1][1], acc[3][1]);
    const float r0 = tr16(a0, a2) + mlp[0], r1 = tr16(a1, a3) + mlp[1];
    if (cact) ST16(r0, r1, WS_FG + b * 3584 + col * 8);
    CBAR();
    flag[12 + kw] = 1u;
  } else {
    const int vw = wave - 8;
    __builtin_amdgcn_s_setprio(1);
    const int ks = lane >> 3, c = lane & 7;
    const auto rv = MKRS(vc + ((size_t)g * SK + (SK - WIN)) * DH, WIN * DH * 2);
    const unsigned vo = ks * 512 + vw * 128 + c * 16;
    const unsigned ooff = WS_OG + ((g * 16 + ((lane >> 5) * 8 + 2 * vw + (c >> 2))) * 32 + s) * 128 +
                          ((c & 3) * 4 + 2 * ((lane >> 4) & 1) + ((lane >> 3) & 1)) * 8;
    v4u vr[16];
    __builtin_amdgcn_s_barrier();
    SB();
    lds_wait1<true>(flag + 16 + vw);
#pragma unroll
    for (int i = 0; i < 16; ++i) {
      if (i >= WIN_V && i < 10) { if (!RELEASED()) VMCNT(WIN_V - 1); }
      vr[i] = NTLD(rv, vo, CHUNK(i) * 4096); SB();
    }
    flag[28 + vw] = 1u;
    SB();
    __builtin_amdgcn_s_setprio(3);
    v4u fr[2][8];
#define VTRANS(bt, jj) _Pragma("unroll") for (int k = 0; k < 4; ++k) { \
      fr[bt][2 * k][jj] = __builtin_amdgcn_perm(vr[8 * bt + 2 * jj + 1][k], vr[8 * bt + 2 * jj][k], 0x05040100u); \
      fr[bt][2 * k + 1][jj] = __builtin_amdgcn_perm(vr[8 * bt + 2 * jj + 1][k], vr[8 * bt + 2 * jj][k], 0x07060302u); }
    VTRANS(0, 0) SB(); VTRANS(0, 1) SB(); VTRANS(0, 2) SB(); VTRANS(0, 3) SB();
    lds_wait4<true>(flag + 4);
    const unsigned tag = flag[26];
    bf16x8 pa[2];
    {
      const int m = lane & 15, r = lane >> 4, h = m >> 3, set = m & 1;
      const v4f wm0 = *(const LAS v4f*)(lds + L_WST), wm1 = *(const LAS v4f*)(lds + L_WST + 4);
      const float mk = u2f(lds[L_WST + r * 2 + h]);
      const LAS v4f* pp = (const LAS v4f*)(lds + L_P + ((2 * r + set) * 2 + h) * 20);
      const v4f p0 = pp[0], p1 = pp[1], p2 = pp[2], p3 = pp[3];
      const float M = h ? fmaxf(fmaxf(wm0[1], wm0[3]), fmaxf(wm1[1], wm1[3])) : fmaxf(fmaxf(wm0[0], wm0[2]), fmaxf(wm1[0], wm1[2]));
      const float f = (m & 2) ? 0.f : __builtin_amdgcn_exp2f((mk - M) * L2E);
      const v4u a0 = {pack_bf16(p0[0] * f, p0[1] * f), pack_bf16(p0[2] * f, p0[3] * f), pack_bf16(p1[0] * f, p1[1] * f), pack_bf16(p1[2] * f, p1[3] * f)};
      const v4u a1 = {pack_bf16(p2[0] * f, p2[1] * f), pack_bf16(p2[2] * f, p2[3] * f), pack_bf16(p3[0] * f, p3[1] * f), pack_bf16(p3[2] * f, p3[3] * f)};
      pa[0] = __builtin_bit_cast(bf16x8, a0); pa[1] = __builtin_bit_cast(bf16x8, a1);
    }
    v4f acc[8];
#pragma unroll
    for (int d = 0; d < 8; ++d)
      acc[d] = __builtin_amdgcn_mfma_f32_16x16x32_bf16(pa[0], __builtin_bit_cast(bf16x8, fr[0][d]), v4f{0.f, 0.f, 0.f, 0.f}, 0, 0, 0);
    SB();
    VTRANS(1, 0) SB(); VTRANS(1, 1) SB(); VTRANS(1, 2) SB(); VTRANS(1, 3)
#pragma unroll
    for (int d = 0; d < 8; ++d)
      acc[d] = __builtin_amdgcn_mfma_f32_16x16x32_bf16(pa[1], __builtin_bit_cast(bf16x8, fr[1][d]), acc[d], 0, 0, 0);
    const bool b4 = lane & 16, b3 = lane & 8;
    float t4[4];
#pragma unroll
    for (int k = 0; k < 4; ++k) {
      const float e0 = b4 ? acc[k + 4][0] : acc[k][0], e1 = b4 ? acc[k + 4][1] : acc[k][1];
      t4[k] = b3 ? e1 : e0;
    }
    const float r0 = trrow<ROR8>(t4[0], t4[2], b3), r1 = trrow<ROR8>(t4[1], t4[3], b3);
    __builtin_amdgcn_raw_buffer_store_b64(v2u{pack_bf16(r0, r1), tag}, rs, ooff, 0, 16);
    CBAR();
    flag[8 + vw] = 1u;
  }
  asm volatile("" ::"v"(pf_));
}

extern "C" void kernel_launch(void* const* d_in, const int* in_sizes, int n_in, void* d_out, int out_size, void* d_ws, size_t ws_size,
                              hipStream_t stream) {
  fused_decode<<<256, 768, 0, stream>>>((const u16*)d_in[0], (const u16*)d_in[1], (const u16*)d_in[2], (const u16*)d_in[3],
                                        (const u16*)d_in[4], (const u16*)d_in[6], (const u16*)d_in[7], (const u16*)d_in[8],
                                        (const u16*)d_in[9], (u16*)d_out, (unsigned char*)d_ws);
}
```

```cpp
#include <hip/hip_runtime.h>
#include <hip/hip_bf16.h>
#include <stdint.h>

#define GAS __attribute__((address_space(1)))
#define LAS __attribute__((address_space(3)))
typedef unsigned short u16;
typedef unsigned v4u __attribute__((ext_vector_type(4)));
typedef unsigned v2u __attribute__((ext_vector_type(2)));
typedef float v4f __attribute__((ext_vector_type(4)));
typedef float v2f __attribute__((ext_vector_type(2)));
typedef __bf16 bf16x2 __attribute__((ext_vector_type(2)));
typedef __bf16 bf16x8 __attribute__((ext_vector_type(8)));
typedef GAS unsigned gu32;
#define RLX_AGENT __ATOMIC_RELAXED, __HIP_MEMORY_SCOPE_AGENT
#define SB() __builtin_amdgcn_sched_barrier(0)
#define CBAR() asm volatile("" ::: "memory")
#define VMCNT(n) asm volatile("s_waitcnt vmcnt(%0)" ::"i"(n) : "memory")

#define WIN_K 4
#define WIN_V 2
#define QDEAD 210
#define QMIN 70
#define ODELAY 60
#define FDELAY 50
#define CODEPF 16000

constexpr int D = 448, SK = 32768, DH = 256, WIN = 4096, F = 1792;
constexpr float L2E = 1.4426950408889634f;
constexpr unsigned SPIN_G = 1u << 17;
constexpr unsigned SPIN_L = 1u << 21;

constexpr unsigned WS_EPOCH = 0;
constexpr unsigned WS_QG = 4096;
constexpr unsigned WS_SG = WS_QG + 65536;
constexpr unsigned WS_ZG = WS_SG + 32768;
constexpr unsigned WS_OG = 262144;
constexpr unsigned WS_FG = WS_OG + (512u << 10);
constexpr unsigned WS_END = WS_FG + 256u * 448u * 8u;

constexpr int L_Q = 32;
constexpr int L_P = L_Q + 256;
constexpr int L_WST = L_P + 16 * 20;
constexpr int L_QP = L_WST + 16;
constexpr int L_O = L_QP + 32;
constexpr int L_MLP = L_O + 16;
constexpr int L_END = L_MLP + 448;

__device__ __forceinline__ float u2f(unsigned v) { return __builtin_bit_cast(float, v); }
__device__ __forceinline__ unsigned f2u(float v) { return __builtin_bit_cast(unsigned, v); }
__device__ __forceinline__ float lo2f(unsigned v) { return u2f(v << 16); }
__device__ __forceinline__ float hi2f(unsigned v) { return u2f(v & 0xffff0000u); }
__device__ __forceinline__ unsigned pack_bf16(float lo, float hi) {
  bf16x2 r; r[0] = (__bf16)lo; r[1] = (__bf16)hi; return __builtin_bit_cast(unsigned, r);
}
__device__ __forceinline__ float rbf(float f) { return (float)(__bf16)f; }
__device__ __forceinline__ float dot2(unsigned a, unsigned b, float c) {
  return __builtin_amdgcn_fdot2_f32_bf16(__builtin_bit_cast(bf16x2, a), __builtin_bit_cast(bf16x2, b), c, false);
}
__device__ __forceinline__ v2f pkfma(v2f a, v2f b, v2f c) { return __builtin_elementwise_fma(a, b, c); }
template <int CTRL> __device__ __forceinline__ float dpp(float x) {
  return u2f((unsigned)__builtin_amdgcn_mov_dpp((int)f2u(x), CTRL, 0xf, 0xf, true));
}
constexpr int ROR8 = 0x128, ROR4 = 0x124, ROR2 = 0x122, ROR1 = 0x121, XOR7 = 0x141, XOR2 = 0x4E, XOR1 = 0xB1;
__device__ __forceinline__ float tr32(float a, float b) {
  auto r = __builtin_amdgcn_permlane32_swap(f2u(a), f2u(b), false, false); return u2f(r[0]) + u2f(r[1]);
}
__device__ __forceinline__ float tr16(float a, float b) {
  auto r = __builtin_amdgcn_permlane16_swap(f2u(a), f2u(b), false, false); return u2f(r[0]) + u2f(r[1]);
}
__device__ __forceinline__ float rowsum(float v) { v += dpp<ROR8>(v); v += dpp<ROR4>(v); v += dpp<ROR2>(v); v += dpp<ROR1>(v); return v; }
__device__ __forceinline__ float rowmax(float v) {
  v = fmaxf(v, dpp<ROR8>(v)); v = fmaxf(v, dpp<ROR4>(v)); v = fmaxf(v, dpp<ROR2>(v)); v = fmaxf(v, dpp<ROR1>(v)); return v;
}
__device__ __forceinline__ float max16(float v) {
  auto r = __builtin_amdgcn_permlane16_swap(f2u(v), f2u(v), false, false); return fmaxf(u2f(r[0]), u2f(r[1]));
}
__device__ __forceinline__ float max32(float v) {
  auto r = __builtin_amdgcn_permlane32_swap(f2u(v), f2u(v), false, false); return fmaxf(u2f(r[0]), u2f(r[1]));
}
template <int CTRL> __device__ __forceinline__ float trrow(float a, float b, bool bit) {
  const float keep = bit ? b : a, send = bit ? a : b;
  return keep + dpp<CTRL>(send);
}
#define MKRS(p, bytes) __builtin_amdgcn_make_buffer_rsrc((void*)(p), 0, (bytes), 0x00020000)
#define NTLD(rsrc, voff, soff) __builtin_amdgcn_raw_buffer_load_b128((rsrc), (voff), (soff), 2)

__device__ __forceinline__ void ln_stats8(const float (&v)[8], bool act, float& m, float& inv) {
  const float c = u2f(__builtin_amdgcn_readfirstlane(f2u(v[0])));
  float d[8];
#pragma unroll
  for (int i = 0; i < 8; ++i) d[i] = v[i] - c;
  float s = ((d[0] + d[1]) + (d[2] + d[3])) + ((d[4] + d[5]) + (d[6] + d[7]));
  float q = fmaf(d[0], d[0], fmaf(d[1], d[1], fmaf(d[2], d[2], d[3] * d[3]))) + fmaf(d[4], d[4], fmaf(d[5], d[5], fmaf(d[6], d[6], d[7] * d[7])));
  s = act ? s : 0.f; q = act ? q : 0.f;
  float t = tr32(s, q);
  t = tr16(t, t);
  t = rowsum(t);
  const float S = u2f(__builtin_amdgcn_readlane(f2u(t), 0)) * (1.f / D), Q = u2f(__builtin_amdgcn_readlane(f2u(t), 32)) * (1.f / D);
  const float var = rbf(fmaxf(fmaf(-S, S, Q), 0.f));
  inv = 1.f / rbf(sqrtf(rbf(var + 1e-5f)));
  m = rbf(c + S);
}
__device__ __forceinline__ float ln_apply(float v, float m, float inv) { return rbf(rbf(v - m) * inv); }

__device__ __forceinline__ void gemv32(const float (&xv)[7], const v4u (&w)[7], float& c0, float& c1) {
  v2f acc[4];
#pragma unroll
  for (int k = 0; k < 4; ++k) acc[k] = v2f{0.f, 0.f};
#pragma unroll
  for (int i = 0; i < 7; ++i) {
#pragma unroll
    for (int k = 0; k < 4; ++k) acc[k] = pkfma(v2f{lo2f(w[i][k]), hi2f(w[i][k])}, v2f{xv[i], xv[i]}, acc[k]);
    SB();
  }
  const float a0 = tr32(acc[0][0], acc[2][0]), a1 = tr32(acc[0][1], acc[2][1]);
  const float a2 = tr32(acc[1][0], acc[3][0]), a3 = tr32(acc[1][1], acc[3][1]);
  c0 = tr16(a0, a2); c1 = tr16(a1, a3);
  c0 += dpp<ROR8>(c0); c1 += dpp<ROR8>(c1);
  c0 += dpp<ROR4>(c0); c1 += dpp<ROR4>(c1);
}

template <bool NAP> __device__ __forceinline__ void lds_wait1(volatile LAS unsigned* p) {
#pragma unroll 1
  for (unsigned n = 0; n < SPIN_L; ++n) { if (*p == 1u) break; if (NAP) __builtin_amdgcn_s_sleep(1); }
  CBAR();
}
__device__ __forceinline__ void lds_wait2(volatile LAS unsigned* p) {
#pragma unroll 1
  for (unsigned n = 0; n < SPIN_L; ++n) { const v2u f = *(volatile LAS v2u*)p; if ((f[0] & f[1]) == 1u) break; __builtin_amdgcn_s_sleep(1); }
  CBAR();
}
template <bool NAP> __device__ __forceinline__ void lds_wait4(volatile LAS unsigned* p) {
#pragma unroll 1
  for (unsigned n = 0; n < SPIN_L; ++n) {
    const v4u f = *(volatile LAS v4u*)p;
    if ((f[0] & f[1] & f[2] & f[3]) == 1u) break;
    if (NAP) __builtin_amdgcn_s_sleep(1);
  }
  CBAR();
}
__device__ __forceinline__ void wait_ticks(unsigned long long t0, unsigned ticks) {
#pragma unroll 1
  while (__builtin_amdgcn_s_memrealtime() - t0 < ticks) __builtin_amdgcn_s_sleep(1);
}
#define LOAD_EPOCH() __hip_atomic_load((gu32*)(ws + WS_EPOCH), RLX_AGENT)
#define TAGOK(v) ((v)[1] == tag && (v)[3] == tag)
#define LD16(off) __builtin_amdgcn_raw_buffer_load_b128(rs, (off), 0, 16)
#define ST16(a, c, off) __builtin_amdgcn_raw_buffer_store_b128(v4u{f2u(a), tag, f2u(c), tag}, rs, (off), 0, 16)
#define RELEASED() ({ const v2u f_ = *(volatile LAS v2u*)(flag + 24); __builtin_amdgcn_readfirstlane(f_[0] & f_[1]) == 1u; })
#define CHUNK(u) ((((u) + 2 * g) & 15) * 32 + ((s + 5 * (u) + 3 * g) & 31))

__global__ void __launch_bounds__(768, 3)
fused_decode(const u16* __restrict__ x, const u16* __restrict__ kc, const u16* __restrict__ vc, const u16* __restrict__ wq,
             const u16* __restrict__ wo, const u16* __restrict__ w1, const u16* __restrict__ b1, const u16* __restrict__ w2,
             const u16* __restrict__ b2, u16* __restrict__ out, unsigned char* ws) {
  __shared__ __attribute__((aligned(16))) unsigned lds_[L_END];
  LAS unsigned* lds = (LAS unsigned*)lds_;
  volatile LAS unsigned* flag = (volatile LAS unsigned*)lds;
  const int tid = threadIdx.x, lane = tid & 63, wave = __builtin_amdgcn_readfirstlane(tid >> 6);
  const int b = blockIdx.x, g = b & 7, s = b >> 3;
  unsigned pf_ = 0;
  {
    const unsigned long long pc = __builtin_amdgcn_s_getpc() & ~255ull;
    const unsigned off = tid * 64u;
    if (off < CODEPF) pf_ = *(const GAS unsigned*)(pc + off);
  }
  asm volatile("s_waitcnt lgkmcnt(0)" :
               : "s"(x), "s"(kc), "s"(vc), "s"(wq), "s"(wo), "s"(w1), "s"(b1), "s"(w2), "s"(b2), "s"(out), "s"(ws) : "memory");
  const auto rs = MKRS(ws, WS_END);

  if (__builtin_expect(wave < 4, 1)) {
    __builtin_amdgcn_s_setprio(3);
    if (__builtin_expect(wave < 2, 1)) {
      float xv[8], xr[7]; v4u w[7]; u16 xb[7];
      const int row0 = (s >> 4) * 224 + wave * 112 + (lane >> 2);
      const v4u xa = *reinterpret_cast<const v4u*>(x + (lane < 56 ? lane : 55) * 8);
#pragma unroll
      for (int i = 0; i < 7; ++i) xb[i] = x[row0 + 16 * i];
      const unsigned ep = LOAD_EPOCH();
      SB();
      const auto rw = MKRS(wq, D * 4096 * 2);
      const unsigned wo_ = (row0 * 4096 + g * 512 + (s & 15) * 32 + (lane & 3) * 8) * 2;
#pragma unroll
      for (int i = 0; i < 7; ++i) { w[i] = NTLD(rw, wo_, i * 16 * 4096 * 2); SB(); }
      __builtin_amdgcn_s_barrier();
      SB();
      const unsigned tag = ep + 1u;
#pragma unroll
      for (int k = 0; k < 4; ++k) { xv[2 * k] = lo2f(xa[k]); xv[2 * k + 1] = hi2f(xa[k]); }
      asm volatile("" : "+v"(xv[0]));
      SB();
      const unsigned long long t_x = __builtin_amdgcn_s_memrealtime();
      SB();
      if (wave == 0) flag[26] = tag;
      float m, inv;
      ln_stats8(xv, lane < 56, m, inv);
#pragma unroll
      for (int i = 0; i < 7; ++i) xr[i] = ln_apply(lo2f(xb[i]), m, inv);
      SB();
      float c0, c1;
      gemv32(xr, w, c0, c1);
      const int cpair = 4 * (lane & 3) + (lane >> 4);
      if (wave == 1) {
        if ((lane & 12) == 0) *(LAS v2f*)(lds + L_QP + 2 * cpair) = v2f{c0, c1};
        CBAR();
        flag[2] = 1u;
      } else {
        lds_wait1<false>(flag + 2);
        const v2f o = *(const LAS v2f*)(lds + L_QP + 2 * cpair);
        if ((lane & 12) == 0) ST16(c0 + o[0], c1 + o[1], WS_QG + ((g * 2 + (s >> 4)) * 512 + (s & 15) * 32 + 2 * cpair) * 8);
      }
      wait_ticks(__builtin_amdgcn_s_memrealtime(), QMIN);
      wait_ticks(t_x, QDEAD);
      {
        const unsigned qoff = WS_QG + (g * 1024 + wave * 256 + lane * 4) * 8;
        v4u a0 = LD16(qoff), a1 = LD16(qoff + 16), a2 = LD16(qoff + 4096), a3 = LD16(qoff + 4096 + 16);
        SB();
        flag[24 + wave] = 1u;
        SB();
#pragma unroll 1
        for (unsigned n = 0; n < SPIN_G; ++n) {
          const bool k0 = TAGOK(a0) && TAGOK(a1), k1 = TAGOK(a2) && TAGOK(a3);
          if (__all(k0 && k1)) break;
          CBAR();
          if (!k0) { a0 = LD16(qoff); a1 = LD16(qoff + 16); }
          if (!k1) { a2 = LD16(qoff + 4096); a3 = LD16(qoff + 4096 + 16); }
        }
        *(LAS v2u*)(lds + L_Q + wave * 128 + lane * 2) =
            v2u{pack_bf16(u2f(a0[0]) + u2f(a2[0]), u2f(a0[2]) + u2f(a2[2])), pack_bf16(u2f(a1[0]) + u2f(a3[0]), u2f(a1[2]) + u2f(a3[2]))};
        CBAR();
        flag[wave] = 1u;
      }
      if (wave == 0) {
        lds_wait4<true>(flag + 28);
        lds_wait4<false>(flag + 8);
        const unsigned long long t_pub = __builtin_amdgcn_s_memrealtime();
        const unsigned soff = WS_SG + (g * 32 + (lane >> 2)) * 128 + (s >> 4) * 16;
        const unsigned ooff = WS_OG + ((g * 16 + (s >> 1)) * 32 + (lane >> 2)) * 128 + (s & 1) * 64 + (lane & 3) * 16;
        v4u st0 = LD16(soff), st1 = LD16(soff + 2048);
#pragma unroll 1
        for (unsigned n = 0; n < SPIN_G; ++n) {
          const bool k0 = TAGOK(st0), k1 = TAGOK(st1);
          if (__all(k0 && k1)) break;
          CBAR();
          if (!k0) st0 = LD16(soff);
          if (!k1) st1 = LD16(soff + 2048);
        }
        float cA, cB;
        {
          float M = fmaxf(u2f(st0[0]), u2f(st1[0]));
          M = fmaxf(M, dpp<ROR4>(M)); M = fmaxf(M, dpp<ROR8>(M));
          M = max32(max16(M));
          cA = __builtin_amdgcn_exp2f((u2f(st0[0]) - M) * L2E); cB = __builtin_amdgcn_exp2f((u2f(st1[0]) - M) * L2E);
          float L = fmaf(cA, u2f(st0[2]), cB * u2f(st1[2]));
          L = tr32(L, L); L = tr16(L, L); L += dpp<ROR8>(L); L += dpp<ROR4>(L);
          const float il = 1.f / L;
          cA *= il; cB *= il;
          asm volatile("" : "+v"(cA), "+v"(cB));
        }
        SB();
        wait_ticks(t_pub, ODELAY);
        v4u oa = LD16(ooff), ob = LD16(ooff + 2048);
        SB();
        flag[21] = 1u;
        SB();
#pragma unroll 1
        for (unsigned n = 0; n < SPIN_G; ++n) {
          const bool k0 = TAGOK(oa), k1 = TAGOK(ob);
          if (__all(k0 && k1)) break;
          CBAR();
          if (!k0) oa = LD16(ooff);
          if (!k1) ob = LD16(ooff + 2048);
        }
        const float e0 = fmaf(cA, lo2f(oa[0]), cB * lo2f(ob[0])), e1 = fmaf(cA, hi2f(oa[0]), cB * hi2f(ob[0]));
        const float e2 = fmaf(cA, lo2f(oa[2]), cB * lo2f(ob[2])), e3 = fmaf(cA, hi2f(oa[2]), cB * hi2f(ob[2]));
        float t = tr16(tr32(e0, e2), tr32(e1, e3));
        t += dpp<ROR8>(t);
        t += dpp<ROR4>(t);
        if ((lane & 12) == 0) lds[L_O + 4 * (lane & 3) + (lane >> 4)] = f2u(rbf(t));
        CBAR();
        flag[20] = 1u;
      }
    } else if (wave == 2) {
      const int u = s >> 2, qd = s & 3;
      if (u < 7) {
        const int cb = g * 7 + u;
        float xv[8], xr[7]; v4u w[7]; u16 xb[7];
        const int row0 = qd * 112 + (lane >> 2);
        const v4u xa = *reinterpret_cast<const v4u*>(x + (lane < 56 ? lane : 55) * 8);
#pragma unroll
        for (int i = 0; i < 7; ++i) xb[i] = x[row0 + 16 * i];
        const unsigned ep = LOAD_EPOCH();
        const unsigned bb = *reinterpret_cast<const unsigned*>(b1 + cb * 32 + qd * 8 + 2 * (lane & 3));
        SB();
        __builtin_amdgcn_s_barrier();
        SB();
        const auto rw = MKRS(w1, D * F * 2);
        const unsigned wo_ = (row0 * F + cb * 32 + (lane & 3) * 8) * 2;
#pragma unroll
        for (int i = 0; i < 7; ++i) { w[i] = NTLD(rw, wo_, i * 16 * F * 2); SB(); }
        __builtin_amdgcn_s_setprio(1);
        const unsigned tag = ep + 1u;
#pragma unroll
        for (int k = 0; k < 4; ++k) { xv[2 * k] = lo2f(xa[k]); xv[2 * k + 1] = hi2f(xa[k]); }
        float m, inv, m2, inv2;
        ln_stats8(xv, lane < 56, m, inv);
#pragma unroll
        for (int i = 0; i < 8; ++i) xv[i] = ln_apply(xv[i], m, inv);
        ln_stats8(xv, lane < 56, m2, inv2);
#pragma unroll
        for (int i = 0; i < 7; ++i) xr[i] = ln_apply(ln_apply(lo2f(xb[i]), m, inv), m2, inv2);
        SB();
        float c0, c1;
        gemv32(xr, w, c0, c1);
        if ((lane & 12) == 0) ST16(c0, c1, WS_ZG + ((cb * 4 + qd) * 32 + 2 * (4 * (lane & 3) + (lane >> 4))) * 8);
        lds_wait2(flag);
        const unsigned zoff = WS_ZG + ((cb * 4 + ((lane >> 2) & 3)) * 32 + qd * 8 + 2 * (lane & 3)) * 8;
        v4u z;
#pragma unroll 1
        for (unsigned n = 0; n < SPIN_G; ++n) {
          CBAR();
          z = LD16(zoff);
          if (__all(TAGOK(z))) break;
          __builtin_amdgcn_s_sleep(8);
        }
        float z0 = u2f(z[0]), z1 = u2f(z[2]);
        z0 += dpp<ROR8>(z0); z1 += dpp<ROR8>(z1);
        z0 += dpp<ROR4>(z0); z1 += dpp<ROR4>(z1);
        z0 = rbf(rbf(z0) + lo2f(bb));
        z1 = rbf(rbf(z1) + hi2f(bb));
        const float u0 = 0.7978845608028654f * (z0 + 0.044715f * z0 * z0 * z0);
        const float u1 = 0.7978845608028654f * (z1 + 0.044715f * z1 * z1 * z1);
        const float t0 = 1.f - 2.f / (__builtin_amdgcn_exp2f(2.f * L2E * u0) + 1.f);
        const float t1 = 1.f - 2.f / (__builtin_amdgcn_exp2f(2.f * L2E * u1) + 1.f);
        const float h0 = rbf(0.5f * z0 * (1.f + t0)), h1 = rbf(0.5f * z1 * (1.f + t1));
        lds_wait4<true>(flag + 28);
        v4u r2[8];
        const auto rw2 = MKRS(w2, D * F * 2);
        const unsigned w2o = ((cb * 32 + qd * 8) * D + (lane < 56 ? lane : 55) * 8) * 2;
#pragma unroll
        for (int r = 0; r < 8; ++r) { r2[r] = NTLD(rw2, w2o, r * D * 2); SB(); }
        v2f acc[4];
#pragma unroll
        for (int k = 0; k < 4; ++k) acc[k] = v2f{0.f, 0.f};
#pragma unroll
        for (int r = 0; r < 8; ++r) {
          const float hr = u2f(__builtin_amdgcn_readlane(f2u((r & 1) ? h1 : h0), r >> 1));
#pragma unroll
          for (int k = 0; k < 4; ++k) acc[k] = pkfma(v2f{lo2f(r2[r][k]), hi2f(r2[r][k])}, v2f{hr, hr}, acc[k]);
          SB();
        }
        if (lane < 56) {
          LAS v4f* mp = (LAS v4f*)(lds + L_MLP + lane * 8);
          mp[0] = v4f{acc[0][0], acc[0][1], acc[1][0], acc[1][1]};
          mp[1] = v4f{acc[2][0], acc[2][1], acc[3][0], acc[3][1]};
        }
      } else {
        __builtin_amdgcn_s_barrier();
        if (lane < 56) {
          LAS v4f* mp = (LAS v4f*)(lds + L_MLP + lane * 8);
          mp[0] = v4f{0.f, 0.f, 0.f, 0.f};
          mp[1] = v4f{0.f, 0.f, 0.f, 0.f};
        }
      }
      CBAR();
      flag[3] = 1u;
    } else {
      const int ell = (s >> 3) * 8 + g;
      if (lane < 32) flag[lane] = 0u;
      asm volatile("s_waitcnt lgkmcnt(0)" ::: "memory");
      __builtin_amdgcn_s_barrier();
      if (ell < 28) {
        const int cp = ell * 8 + (s & 7);
        const float base = lo2f(x[2 * cp + (lane >> 5)]) + lo2f(b2[2 * cp + (lane >> 5)]);
        lds_wait1<true>(flag + 21);
        lds_wait4<false>(flag + 12);
        const unsigned tag = flag[26];
        wait_ticks(__builtin_amdgcn_s_memrealtime(), FDELAY);
        const unsigned foff = WS_FG + lane * 3584 + cp * 16;
        v4u f0 = LD16(foff), f1 = LD16(foff + 64 * 3584), f2 = LD16(foff + 128 * 3584), f3 = LD16(foff + 192 * 3584);
#pragma unroll 1
        for (unsigned n = 0; n < SPIN_G; ++n) {
          const bool k0 = TAGOK(f0), k1 = TAGOK(f1), k2 = TAGOK(f2), k3 = TAGOK(f3);
          if (__all(k0 && k1 && k2 && k3)) break;
          CBAR();
          if (!k0) f0 = LD16(foff);
          if (!k1) f1 = LD16(foff + 64 * 3584);
          if (!k2) f2 = LD16(foff + 128 * 3584);
          if (!k3) f3 = LD16(foff + 192 * 3584);
        }
        const float s0 = (u2f(f0[0]) + u2f(f1[0])) + (u2f(f2[0]) + u2f(f3[0]));
        const float s1 = (u2f(f0[2]) + u2f(f1[2])) + (u2f(f2[2]) + u2f(f3[2]));
        float t = tr32(s0, s1);
        t = tr16(t, t);
        t = rowsum(t);
        if ((lane & 31) == 0) {
          const __bf16 r = (__bf16)(base + t);
          out[2 * cp + (lane >> 5)] = __builtin_bit_cast(u16, r);
          if (b == 0 && lane == 0) __hip_atomic_store((gu32*)(ws + WS_EPOCH), tag, RLX_AGENT);
        }
      }
    }
  } else if (__builtin_expect(wave < 8, 1)) {
    const int kw = wave - 4;
    __builtin_amdgcn_s_setprio(2);
    const auto rk = MKRS(kc + ((size_t)g * SK + (SK - WIN)) * DH, WIN * DH * 2);
    const unsigned ko = kw * 1024 + lane * 16;
    v4u kr[16];
    __builtin_amdgcn_s_barrier();
    SB();
#pragma unroll
    for (int i = 0; i < 16; ++i) {
      if (i >= WIN_K) { if (!RELEASED()) VMCNT(WIN_K - 1); }
      kr[i] = NTLD(rk, ko, CHUNK(i) * 4096); SB();
    }
    flag[16 + kw] = 1u;
    SB();
    lds_wait2(flag);
    const unsigned tag = flag[26];
    const v4u q0 = *(const LAS v4u*)(lds + L_Q + (lane & 31) * 4);
    const v4u q1 = *(const LAS v4u*)(lds + L_Q + 128 + (lane & 31) * 4);
    float y[16];
#pragma unroll
    for (int i = 0; i < 16; ++i) {
      float p0 = 0.f, p1 = 0.f;
#pragma unroll
      for (int k = 0; k < 4; ++k) { p0 = dot2(kr[i][k], q0[k], p0); p1 = dot2(kr[i][k], q1[k], p1); }
      y[i] = tr16(p0, p1);
      if ((i & 3) == 3) SB();
    }
    const bool b3 = lane & 8, b2_ = lane & 4, b1_ = lane & 2, b0_ = lane & 1;
#pragma unroll
    for (int i = 0; i < 8; ++i) y[i] = trrow<ROR8>(y[i], y[i + 8], b3);
#pragma unroll
    for (int i = 0; i < 4; ++i) y[i] = trrow<XOR7>(y[i], y[i + 4], b2_);
#pragma unroll
    for (int i = 0; i < 2; ++i) y[i] = trrow<XOR2>(y[i], y[i + 2], b1_);
    const float sc = rbf(trrow<XOR1>(y[0], y[1], b0_)) * 0.0625f;
    const float m = max32(rowmax(sc));
    const float p = __builtin_amdgcn_exp2f((sc - m) * L2E);
    {
      const int h = (lane >> 4) & 1;
      lds[L_P + ((2 * kw + (lane >> 5)) * 2 + h) * 20 + (lane & 15)] = f2u(p);
      if ((lane & 47) == 0) lds[L_WST + kw * 2 + h] = f2u(m);
      float l = rowsum(p);
      l = tr32(l, l);
      if ((lane & 47) == 0) lds[L_WST + 8 + kw * 2 + h] = f2u(l);
    }
    CBAR();
    flag[4 + kw] = 1u;
    if (kw == 0) {
      lds_wait4<true>(flag + 4);
      const int h = lane & 1;
      float mm[4], ll[4];
#pragma unroll
      for (int k = 0; k < 4; ++k) { mm[k] = u2f(lds[L_WST + k * 2 + h]); ll[k] = u2f(lds[L_WST + 8 + k * 2 + h]); }
      const float M = fmaxf(fmaxf(mm[0], mm[1]), fmaxf(mm[2], mm[3]));
      float L = 0.f;
#pragma unroll
      for (int k = 0; k < 4; ++k) L = fmaf(__builtin_amdgcn_exp2f((mm[k] - M) * L2E), ll[k], L);
      if (lane < 2) ST16(M, L, WS_SG + (g * 32 + s) * 128 + h * 16);
    }
    const int r4 = lane >> 4, c16 = lane & 15;
    const bool cact = kw < 3 || c16 < 8;
    const int cbase = kw * 128 + (cact ? c16 : (c16 & 7)) * 8;
    const int col = cbase + 2 * r4;
    const auto rwo = MKRS(wo, 4096 * D * 2);
    const unsigned woo = ((g * 512 + s * 16 + 4 * r4) * D + cbase) * 2;
    v4u wr[4];
    lds_wait4<true>(flag + 8);
#pragma unroll
    for (int i = 0; i < 4; ++i) { wr[i] = NTLD(rwo, woo, i * D * 2); SB(); }
    v2f wf[4][4];
#pragma unroll
    for (int i = 0; i < 4; ++i)
#pragma unroll
      for (int k = 0; k < 4; ++k) { wf[i][k] = v2f{lo2f(wr[i][k]), hi2f(wr[i][k])}; asm volatile("" : "+v"(wf[i][k])); }
    __builtin_amdgcn_s_setprio(3);
    lds_wait1<true>(flag + 3);
    const v2f mlp = *(const LAS v2f*)(lds + L_MLP + col);
    lds_wait1<false>(flag + 20);
    const v4f o = *(const LAS v4f*)(lds + L_O + 4 * r4);
    v2f acc[4];
#pragma unroll
    for (int k = 0; k < 4; ++k) acc[k] = wf[0][k] * v2f{o[0], o[0]};
#pragma unroll
    for (int i = 1; i < 4; ++i)
#pragma unroll
      for (int k = 0; k < 4; ++k) acc[k] = pkfma(wf[i][k], v2f{o[i], o[i]}, acc[k]);
    const float a0 = tr32(acc[0][0], acc[2][0]), a1 = tr32(acc[0][1], acc[2][1]);
    const float a2 = tr32(acc[1][0], acc[3][0]), a3 = tr32(acc[1][1], acc[3][1]);
    const float r0 = tr16(a0, a2) + mlp[0], r1 = tr16(a1, a3) + mlp[1];
    if (cact) ST16(r0, r1, WS_FG + b * 3584 + col * 8);
    CBAR();
    flag[12 + kw] = 1u;
  } else {
    const int vw = wave - 8;
    __builtin_amdgcn_s_setprio(1);
    const int ks = lane >> 3, c = lane & 7;
    const auto rv = MKRS(vc + ((size_t)g * SK + (SK - WIN)) * DH, WIN * DH * 2);
    const unsigned vo = ks * 512 + vw * 128 + c * 16;
    const unsigned ooff = WS_OG + ((g * 16 + ((lane >> 5) * 8 + 2 * vw + (c >> 2))) * 32 + s) * 128 +
                          ((c & 3) * 4 + 2 * ((lane >> 4) & 1) + ((lane >> 3) & 1)) * 8;
    v4u vr[16];
    __builtin_amdgcn_s_barrier();
    SB();
    lds_wait1<true>(flag + 16 + vw);
#pragma unroll
    for (int i = 0; i < 16; ++i) {
      if (i >= WIN_V && i < 10) { if (!RELEASED()) VMCNT(WIN_V - 1); }
      vr[i] = NTLD(rv, vo, CHUNK(i) * 4096); SB();
    }
    flag[28 + vw] = 1u;
    SB();
    __builtin_amdgcn_s_setprio(3);
    v4u fr[2][8];
#define VTRANS(bt, jj) _Pragma("unroll") for (int k = 0; k < 4; ++k) { \
      fr[bt][2 * k][jj] = __builtin_amdgcn_perm(vr[8 * bt + 2 * jj + 1][k], vr[8 * bt + 2 * jj][k], 0x05040100u); \
      fr[bt][2 * k + 1][jj] = __builtin_amdgcn_perm(vr[8 * bt + 2 * jj + 1][k], vr[8 * bt + 2 * jj][k], 0x07060302u); }
    VTRANS(0, 0) SB(); VTRANS(0, 1) SB(); VTRANS(0, 2) SB(); VTRANS(0, 3) SB();
    lds_wait4<true>(flag + 4);
    const unsigned tag = flag[26];
    bf16x8 pa[2];
    {
      const int m = lane & 15, r = lane >> 4, h = m >> 3, set = m & 1;
      const v4f wm0 = *(const LAS v4f*)(lds + L_WST), wm1 = *(const LAS v4f*)(lds + L_WST + 4);
      const float mk = u2f(lds[L_WST + r * 2 + h]);
      const LAS v4f* pp = (const LAS v4f*)(lds + L_P + ((2 * r + set) * 2 + h) * 20);
      const v4f p0 = pp[0], p1 = pp[1], p2 = pp[2], p3 = pp[3];
      const float M = h ? fmaxf(fmaxf(wm0[1], wm0[3]), fmaxf(wm1[1], wm1[3])) : fmaxf(fmaxf(wm0[0], wm0[2]), fmaxf(wm1[0], wm1[2]));
      const float f = (m & 2) ? 0.f : __builtin_amdgcn_exp2f((mk - M) * L2E);
      const v4u a0 = {pack_bf16(p0[0] * f, p0[1] * f), pack_bf16(p0[2] * f, p0[3] * f), pack_bf16(p1[0] * f, p1[1] * f), pack_bf16(p1[2] * f, p1[3] * f)};
      const v4u a1 = {pack_bf16(p2[0] * f, p2[1] * f), pack_bf16(p2[2] * f, p2[3] * f), pack_bf16(p3[0] * f, p3[1] * f), pack_bf16(p3[2] * f, p3[3] * f)};
      pa[0] = __builtin_bit_cast(bf16x8, a0); pa[1] = __builtin_bit_cast(bf16x8, a1);
    }
    v4f acc[8];
#pragma unroll
    for (int d = 0; d < 8; ++d)
      acc[d] = __builtin_amdgcn_mfma_f32_16x16x32_bf16(pa[0], __builtin_bit_cast(bf16x8, fr[0][d]), v4f{0.f, 0.f, 0.f, 0.f}, 0, 0, 0);
    SB();
    VTRANS(1, 0) SB(); VTRANS(1, 1) SB(); VTRANS(1, 2) SB(); VTRANS(1, 3)
#pragma unroll
    for (int d = 0; d < 8; ++d)
      acc[d] = __builtin_amdgcn_mfma_f32_16x16x32_bf16(pa[1], __builtin_bit_cast(bf16x8, fr[1][d]), acc[d], 0, 0, 0);
    const bool b4 = lane & 16, b3 = lane & 8;
    float t4[4];
#pragma unroll
    for (int k = 0; k < 4; ++k) {
      const float e0 = b4 ? acc[k + 4][0] : acc[k][0], e1 = b4 ? acc[k + 4][1] : acc[k][1];
      t4[k] = b3 ? e1 : e0;
    }
    const float r0 = trrow<ROR8>(t4[0], t4[2], b3), r1 = trrow<ROR8>(t4[1], t4[3], b3);
    __builtin_amdgcn_raw_buffer_store_b64(v2u{pack_bf16(r0, r1), tag}, rs, ooff, 0, 16);
    CBAR();
    flag[8 + vw] = 1u;
  }
  asm volatile("" ::"v"(pf_));
}

extern "C" void kernel_launch(void* const* d_in, const int* in_sizes, int n_in, void* d_out, int out_size, void* d_ws, size_t ws_size,
                              hipStream_t stream) {
  fused_decode<<<256, 768, 0, stream>>>((const u16*)d_in[0], (const u16*)d_in[1], (const u16*)d_in[2], (const u16*)d_in[3],
                                        (const u16*)d_in[4], (const u16*)d_in[6], (const u16*)d_in[7], (const u16*)d_in[8],
                                        (const u16*)d_in[9], (u16*)d_out, (unsigned char*)d_ws);
}
```
